```python
import torch

def _softmax(x, axis=-1):
    return torch.softmax(x, dim=axis)
def _gelu(x):
    return torch.nn.functional.gelu(x, approximate="tanh")
def _silu(x):
    return torch.nn.functional.silu(x)
def _sigmoid(x):
    return torch.sigmoid(x)
def _layernorm(x, axis=-1, eps=1e-5):
    m = x.mean(dim=axis, keepdim=True)
    v = x.var(dim=axis, keepdim=True, unbiased=False)
    return (x - m) / torch.sqrt(v + eps)
def _scatter_add(tgt, idx, src, axis=0):
    return tgt.clone().index_add_(axis, idx, src)
def _segment_sum(src, seg, n):
    out = torch.zeros((n,) + src.shape[1:], dtype=src.dtype)
    return out.index_add_(0, seg, src)
def _pad(x, pw):
    flat = tuple(v for pair in reversed(pw) for v in pair)
    return torch.nn.functional.pad(x, flat)

kernel_name = 'parallel_sum__sliding_block_cached__dense_activation_gelu__decode'

def setup_inputs():

    _w = (lambda a: (lambda t: t.to(torch.bfloat16) if t.is_floating_point() else t)(torch.as_tensor(a)))
    def _A_setup():
        B, Sq, D = 1, 1, 640
        Hq, Hkv, Sk, Dh = 32, 32, 8192, 128
        assert Hq % Hkv == 0, (Hq, Hkv)
        return {
            "x": _w((lambda *s: torch.randn(*s, dtype=torch.bfloat16) * 0.05)(B, Sq, D)),
            "k_cache": _w((lambda *s: torch.randn(*s, dtype=torch.bfloat16) * 3.2)(B, Hkv, Sk, Dh)),
            "v_cache": _w((lambda *s: torch.randn(*s, dtype=torch.bfloat16) * 0.8)(B, Hkv, Sk, Dh)),
            "wq": _w((lambda *s: torch.randn(*s, dtype=torch.bfloat16) * 3.2)(D, Hq * Dh) * (D ** -0.5)),
            "wo": _w((lambda *s: torch.randn(*s, dtype=torch.bfloat16) * 0.8)(Hq * Dh, D) * ((Hq * Dh) ** -0.5)),
        }
    _A_in = _A_setup()

    def _B_setup():
        B, S, D, F = 1, 1, 640, 1728
        return {
            "x": _w((lambda *s: torch.randn(*s, dtype=torch.bfloat16) * 0.05)(B, S, D)),
            "w1": _w((lambda *s: torch.randn(*s, dtype=torch.bfloat16) * 0.8)(D, F) * (D ** -0.5)),
            "b1": _w((lambda *s: torch.randn(*s, dtype=torch.bfloat16) * 0.8)(F) * 0.01),
            "w2": _w((lambda *s: torch.randn(*s, dtype=torch.bfloat16) * 0.8)(F, D) * (F ** -0.5)),
            "b2": _w((lambda *s: torch.randn(*s, dtype=torch.bfloat16) * 0.8)(D) * 0.01),
        }
    _B_in = _B_setup()

    return {
        "A_x": _A_in["x"],
        "A_k_cache": _A_in["k_cache"],
        "A_v_cache": _A_in["v_cache"],
        "A_wq": _A_in["wq"],
        "A_wo": _A_in["wo"],
        "B_x": _B_in["x"],
        "B_w1": _B_in["w1"],
        "B_b1": _B_in["b1"],
        "B_w2": _B_in["w2"],
        "B_b2": _B_in["b2"],
    }

def reference(A_x, A_k_cache, A_v_cache, A_wq, A_wo, B_x, B_w1, B_b1, B_w2, B_b2):
    def _A_ref(x, k_cache, v_cache, wq, wo):
        B, Sq, D = x.shape
        Hq, Dh = 32, 128
        Hkv, Sk = k_cache.shape[1], k_cache.shape[2]
        W = min(4096, max(256, Sk // 4))
        rep = Hq // Hkv
        q = (lambda x, *d: x.permute(*d))((x @ wq).reshape(B, Sq, Hq, Dh), 0, 2, 1, 3)
        k = (lambda x, n, axis: torch.repeat_interleave(x, n, dim=axis))(k_cache[:, :, Sk - W:, :], rep, axis=1)
        v = (lambda x, n, axis: torch.repeat_interleave(x, n, dim=axis))(v_cache[:, :, Sk - W:, :], rep, axis=1)
        scores = torch.einsum("bhqd,bhkd->bhqk", q, k) * (Dh ** -0.5)
        p = _softmax(scores, axis=-1)
        o = torch.einsum("bhqk,bhkd->bhqd", p, v)
        return (lambda x, *d: x.permute(*d))(o, 0, 2, 1, 3).reshape(B, Sq, Hq * Dh) @ wo

    def _B_ref(x, w1, b1, w2, b2):
        h = torch.einsum("bsd,df->bsf", x, w1) + b1
        h = _gelu(h)
        return torch.einsum("bsf,fd->bsd", h, w2) + b2

    x = A_x
    xn = _layernorm(x, axis=-1)
    return x + _A_ref(xn, A_k_cache, A_v_cache, A_wq, A_wo) + _B_ref(xn, B_w1, B_b1, B_w2, B_b2)
```

```cpp
#include <hip/hip_runtime.h>
#include <hip/hip_bf16.h>

typedef unsigned u32;
typedef unsigned long long u64;
typedef u32 u32x2 __attribute__((ext_vector_type(2)));
typedef u32 u32x4 __attribute__((ext_vector_type(4)));
typedef float f32x2 __attribute__((ext_vector_type(2)));
typedef float f32x4 __attribute__((ext_vector_type(4)));
typedef __bf16 bf2 __attribute__((ext_vector_type(2)));
typedef __attribute__((address_space(1))) u64 gu64;
typedef __amdgpu_buffer_rsrc_t rsrc_t;

#define RLX_AGENT __ATOMIC_RELAXED, __HIP_MEMORY_SCOPE_AGENT
#define AUX_KV 2
#define AUX_W 2
#define AUX_WO 0
#define GQ_SLEEP 0

constexpr int DM = 640, NH = 32, DH = 128, SK = 8192, WIN = 2048, FF = 1728, NQ = NH * DH;
constexpr u32 OOB = 0x80000000u;
constexpr u32 kSpinCap = 1u << 18;
constexpr int WS_QH = 512;
constexpr int WS_OG = WS_QH + 32 * 8 * 192;
constexpr int WS_ML = WS_OG + 32 * 8 * 128;
constexpr int WS_Y = WS_ML + 32 * 16;
constexpr int WS_END = WS_Y + 40 * 32 * 16;

#define ST(i) do { } while (0)

__device__ __forceinline__ rsrc_t mk(const void* p, u32 bytes) {
    return __builtin_amdgcn_make_buffer_rsrc((void*)p, 0, bytes, 0x00020000);
}
__device__ __forceinline__ float dot2(u32 a, u32 b, float c) {
    return __builtin_amdgcn_fdot2_f32_bf16(__builtin_bit_cast(bf2, a), __builtin_bit_cast(bf2, b), c, false);
}
__device__ __forceinline__ u32 bfbits(float f) { return (u32)__builtin_bit_cast(unsigned short, (__bf16)f); }
__device__ __forceinline__ float bfr(float f) { return __uint_as_float(bfbits(f) << 16); }
__device__ __forceinline__ float lo16(u32 v) { return __uint_as_float(v << 16); }
__device__ __forceinline__ float hi16(u32 v) { return __uint_as_float(v & 0xffff0000u); }
template <int C> __device__ __forceinline__ float dpp(float v) {
    return __uint_as_float(__builtin_amdgcn_update_dpp(0u, __float_as_uint(v), C, 0xf, 0xf, true));
}
__device__ __forceinline__ float row_sum(float v) {
    v += dpp<0xB1>(v); v += dpp<0x4E>(v); v += dpp<0x141>(v); v += dpp<0x140>(v); return v;
}
__device__ __forceinline__ float row_max(float v) {
    v = fmaxf(v, dpp<0xB1>(v)); v = fmaxf(v, dpp<0x4E>(v)); v = fmaxf(v, dpp<0x141>(v)); v = fmaxf(v, dpp<0x140>(v)); return v;
}
__device__ __forceinline__ float sw32(float a, float b) {
    auto r = __builtin_amdgcn_permlane32_swap(__float_as_uint(a), __float_as_uint(b), false, false);
    return __uint_as_float(r[0]) + __uint_as_float(r[1]);
}
__device__ __forceinline__ float sw16(float a, float b) {
    auto r = __builtin_amdgcn_permlane16_swap(__float_as_uint(a), __float_as_uint(b), false, false);
    return __uint_as_float(r[0]) + __uint_as_float(r[1]);
}
__device__ __forceinline__ float col_sum(float v) { v = sw32(v, v); return sw16(v, v); }
__device__ __forceinline__ float col_max(float v) {
    auto r = __builtin_amdgcn_permlane32_swap(__float_as_uint(v), __float_as_uint(v), false, false);
    v = fmaxf(__uint_as_float(r[0]), __uint_as_float(r[1]));
    auto s = __builtin_amdgcn_permlane16_swap(__float_as_uint(v), __float_as_uint(v), false, false);
    return fmaxf(__uint_as_float(s[0]), __uint_as_float(s[1]));
}
__device__ __forceinline__ void bar() {
    asm volatile("s_waitcnt lgkmcnt(0)" ::: "memory");
    __builtin_amdgcn_s_barrier();
    asm volatile("" ::: "memory");
}
__device__ __forceinline__ void put(gu64* p, u32 thi, u32 tlo, float v) {
    u32 b = ((__float_as_uint(v) + 0x20u) & ~0x3fu) | tlo;
    __hip_atomic_store(p, ((u64)thi << 32) | b, RLX_AGENT);
}
__device__ __forceinline__ u64 get(gu64* p) { return __hip_atomic_load(p, RLX_AGENT); }
__device__ __forceinline__ bool okg(u64 x, u32 thi, u32 tlo) { return ((((u32)(x >> 32)) ^ thi) | (((u32)x & 0x3fu) ^ tlo)) == 0u; }
__device__ __forceinline__ float val(u64 x) { return __uint_as_float((u32)x & ~0x3fu); }

__global__ void __launch_bounds__(512, 2)
fused_decode(const unsigned short* __restrict__ x, const unsigned short* __restrict__ kc,
             const unsigned short* __restrict__ vc, const unsigned short* __restrict__ wq,
             const unsigned short* __restrict__ wo, const unsigned short* __restrict__ w1,
             const unsigned short* __restrict__ b1, const unsigned short* __restrict__ w2,
             const unsigned short* __restrict__ b2, unsigned short* __restrict__ out, u64* wsp) {
    __shared__ float sA[8][192];
    __shared__ float sG[8][192];
    __shared__ float sO[8][128];
    __shared__ float sOg[8][128];
    __shared__ float sP[48][84];
    __shared__ float sM[8], sL[8];
    __shared__ u32 sH[64];
    gu64* ws = (gu64*)wsp;
    const int tid = threadIdx.x, lane = tid & 63;
    const int w = __builtin_amdgcn_readfirstlane(tid >> 6);
    const int b = blockIdx.x, xcd = b & 7, rr = b >> 3, s4 = rr >> 3;
    const int g = xcd * 4 + s4, j = rr & 7;
    const int f = xcd < 5 ? (s4 < 3 ? 3 * xcd + s4 : -1) : 15 + 4 * (xcd - 5) + s4;
    const bool ffn = f >= 0;
    const int r = lane >> 4, c = lane & 15;
    ST(0);
    const u64 epoch = get(ws);
    const rsrc_t rx = mk(x, DM * 2);
    const u32x4 xa = __builtin_amdgcn_raw_buffer_load_b128(rx, lane * 16, 0, 0);
    const u32x4 xb = __builtin_amdgcn_raw_buffer_load_b128(rx, 1024 + lane * 16, 0, 0);
    const int row0 = 80 * j + 10 * w;
    u32 xr[3]; u32x4 wqv[3]; u32x2 w1v[3];
    const rsrc_t rwq = mk(wq + (size_t)row0 * NQ + g * DH, 9 * NQ * 2 + 256);
    const rsrc_t rw1 = mk(w1 + (size_t)row0 * FF + (ffn ? f : 0) * 64, ffn ? 9 * FF * 2 + 128 : 0);
#pragma unroll
    for (int i = 0; i < 3; ++i) {
        const bool v = i < 2 || r < 2;
        xr[i] = __builtin_amdgcn_raw_buffer_load_b16(rx, v ? (row0 + 4 * i + r) * 2 : OOB, 0, 0);
    }
#pragma unroll
    for (int i = 0; i < 3; ++i) wqv[i] = __builtin_amdgcn_raw_buffer_load_b128(rwq, (4 * i + r) * NQ * 2 + c * 16, 0, AUX_W);
#pragma unroll
    for (int i = 0; i < 3; ++i) w1v[i] = __builtin_amdgcn_raw_buffer_load_b64(rw1, (4 * i + r) * FF * 2 + c * 8, 0, AUX_W);
    const u32 b1v = __builtin_amdgcn_raw_buffer_load_b16(mk(b1 + (ffn ? f : 0) * 64, 128), lane * 2, 0, 0);
    const int ocol = 16 * b + c;
    const u32 xo = __builtin_amdgcn_raw_buffer_load_b16(rx, ocol * 2, 0, 0);
    const u32 b2o = __builtin_amdgcn_raw_buffer_load_b16(mk(b2, DM * 2), ocol * 2, 0, 0);
    const size_t kvoff = ((size_t)g * SK + (SK - WIN) + 256 * j + 32 * w) * DH;
    const rsrc_t rk = mk(kc + kvoff, 8192), rv = mk(vc + kvoff, 8192);
    u32x4 kv[8], vv[8];
#pragma unroll
    for (int i = 0; i < 8; ++i) kv[i] = __builtin_amdgcn_raw_buffer_load_b128(rk, lane * 16, i * 1024, AUX_KV);
    __builtin_amdgcn_sched_barrier(0);

    const u64 tg = epoch + 1;
    const u32 tlo = (u32)tg & 0x3fu, thi = (u32)(tg >> 6);
    float xs[16];
#pragma unroll
    for (int i = 0; i < 4; ++i) { xs[2 * i] = lo16(xa[i]); xs[2 * i + 1] = hi16(xa[i]); xs[8 + 2 * i] = lo16(xb[i]); xs[9 + 2 * i] = hi16(xb[i]); }
    float sum = 0.f;
#pragma unroll
    for (int i = 0; i < 16; ++i) sum += xs[i];
    const float mean = col_sum(row_sum(sum)) * (1.0f / DM);
    float sq = 0.f, sq2 = 0.f;
#pragma unroll
    for (int i = 0; i < 8; ++i) { float d = xs[i] - mean; sq += d * d; float e = xs[8 + i] - mean; sq2 += e * e; }
    sq += lane < 16 ? sq2 : 0.f;
    const float var = bfr(col_sum(row_sum(sq)) * (1.0f / DM));
    const float mb = bfr(mean), sd = bfr(sqrtf(bfr(var + 1e-5f)));
    ST(1);
    float qa[8], ha[4];
#pragma unroll
    for (int k = 0; k < 8; ++k) qa[k] = 0.f;
#pragma unroll
    for (int k = 0; k < 4; ++k) ha[k] = 0.f;
#pragma unroll
    for (int i = 0; i < 3; ++i) {
        u32 xl = bfbits(bfr(lo16(xr[i]) - mb) / sd);
        if (i == 2) xl = r < 2 ? xl : 0u;
        const u32 xh = xl << 16;
#pragma unroll
        for (int k = 0; k < 4; ++k) { qa[2 * k] = dot2(wqv[i][k], xl, qa[2 * k]); qa[2 * k + 1] = dot2(wqv[i][k], xh, qa[2 * k + 1]); }
#pragma unroll
        for (int k = 0; k < 2; ++k) { ha[2 * k] = dot2(w1v[i][k], xl, ha[2 * k]); ha[2 * k + 1] = dot2(w1v[i][k], xh, ha[2 * k + 1]); }
    }
    {
        const float t0 = sw32(qa[0], qa[4]), t1 = sw32(qa[1], qa[5]), t2 = sw32(qa[2], qa[6]), t3 = sw32(qa[3], qa[7]);
        f32x2 u; u[0] = sw16(t0, t2); u[1] = sw16(t1, t3);
        *(f32x2*)&sA[w][8 * c + 2 * r] = u;
        sA[w][128 + 4 * c + r] = sw16(sw32(ha[0], ha[2]), sw32(ha[1], ha[3]));
    }
    bar();
    if (w < 3) {
        float a = 0.f;
#pragma unroll
        for (int k = 0; k < 8; ++k) a += sA[k][tid];
        put(ws + WS_QH + (g * 8 + j) * 192 + tid, thi, tlo, a);
    }
    ST(2);
    if (GQ_SLEEP) __builtin_amdgcn_s_sleep(GQ_SLEEP);
    gu64* gq = ws + WS_QH + (g * 8 + w) * 192 + lane;
    u64 q0 = get(gq), q1 = get(gq + 64), q2 = get(gq + 128);
    __builtin_amdgcn_sched_barrier(0);
#pragma unroll
    for (int i = 0; i < 8; ++i) vv[i] = __builtin_amdgcn_raw_buffer_load_b128(rv, lane * 16, i * 1024, AUX_KV);
    const int r6 = (lane * 26) >> 8, cc = lane - 10 * r6;
    const rsrc_t rwo = mk(wo + (size_t)(g * DH + 16 * w) * DM + 80 * j, 15 * DM * 2 + 160);
    const rsrc_t rw2 = mk(w2 + (size_t)((ffn ? f : 0) * 64 + 8 * w) * DM + 80 * j, ffn ? 7 * DM * 2 + 160 : 0);
    const u32 wofs = lane < 60 ? r6 * DM * 2 + cc * 16 : OOB;
    u32x4 wov[3], w2v[2];
#pragma unroll
    for (int i = 0; i < 3; ++i) wov[i] = __builtin_amdgcn_raw_buffer_load_b128(rwo, wofs + i * 6 * DM * 2, 0, AUX_WO);
#pragma unroll
    for (int i = 0; i < 2; ++i) w2v[i] = __builtin_amdgcn_raw_buffer_load_b128(rw2, wofs + i * 6 * DM * 2, 0, AUX_WO);
    __builtin_amdgcn_sched_barrier(0);
    bool fail = false;
    {
        u32 spins = 0;
        if (__builtin_expect(!__all(okg(q0, thi, tlo) & okg(q1, thi, tlo) & okg(q2, thi, tlo)), 0)) {
            asm volatile("; retry q" ::: "memory");
#pragma nounroll
            do {
                if (++spins > kSpinCap) { fail = true; break; }
                q0 = get(gq); q1 = get(gq + 64); q2 = get(gq + 128);
            } while (!__all(okg(q0, thi, tlo) & okg(q1, thi, tlo) & okg(q2, thi, tlo)));
        }
    }
    sG[w][lane] = val(q0); sG[w][64 + lane] = val(q1); sG[w][128 + lane] = val(q2);
    ST(3);
    bar();
    u32 qb[4];
    {
        f32x2 a = *(const f32x2*)&sG[0][8 * c + 2 * r];
#pragma unroll
        for (int k = 1; k < 8; ++k) a += *(const f32x2*)&sG[k][8 * c + 2 * r];
        const u32 pk = bfbits(a[0]) | (bfbits(a[1]) << 16);
        auto s = __builtin_amdgcn_permlane32_swap(pk, pk, false, false);
        auto s0 = __builtin_amdgcn_permlane16_swap(s[0], s[0], false, false);
        auto s1 = __builtin_amdgcn_permlane16_swap(s[1], s[1], false, false);
        qb[0] = s0[0]; qb[1] = s0[1]; qb[2] = s1[0]; qb[3] = s1[1];
    }
    if (w == 7) {
        float a = 0.f;
#pragma unroll
        for (int k = 0; k < 8; ++k) a += sG[k][128 + lane];
        const float z = bfr(bfr(a) + lo16(b1v));
        const float u = 0.7978845608028654f * (z + 0.044715f * z * z * z);
        const float th = 1.0f - 2.0f / (__expf(2.0f * u) + 1.0f);
        sH[lane] = bfbits(0.5f * z * (1.0f + th));
    }
    float sc[8];
    float mx = -INFINITY;
#pragma unroll
    for (int i = 0; i < 8; ++i) {
        float a = dot2(kv[i][0], qb[0], 0.f);
        a = dot2(kv[i][1], qb[1], a); a = dot2(kv[i][2], qb[2], a); a = dot2(kv[i][3], qb[3], a);
        sc[i] = bfr(bfr(row_sum(a)) * 0.08838834764831845f);
        mx = fmaxf(mx, sc[i]);
    }
    mx = col_max(mx);
    if (lane == 0) sM[w] = mx;
    bar();
    {
        const f32x4 m0 = *(const f32x4*)&sM[0], m1 = *(const f32x4*)&sM[4];
        mx = fmaxf(fmaxf(fmaxf(m0[0], m0[1]), fmaxf(m0[2], m0[3])), fmaxf(fmaxf(m1[0], m1[1]), fmaxf(m1[2], m1[3])));
    }
    float ls = 0.f; u32 pb[8];
#pragma unroll
    for (int i = 0; i < 8; ++i) { const float p = __expf(sc[i] - mx); ls += p; pb[i] = bfbits(p); }
    ls = col_sum(ls);
    if (lane == 0) sL[w] = ls;
    ST(4);
    float oa[8];
#pragma unroll
    for (int k = 0; k < 8; ++k) oa[k] = 0.f;
#pragma unroll
    for (int i = 0; i < 8; ++i) {
        const u32 ph = pb[i] << 16;
#pragma unroll
        for (int k = 0; k < 4; ++k) { oa[2 * k] = dot2(vv[i][k], pb[i], oa[2 * k]); oa[2 * k + 1] = dot2(vv[i][k], ph, oa[2 * k + 1]); }
    }
    {
        const float t0 = sw32(oa[0], oa[4]), t1 = sw32(oa[1], oa[5]), t2 = sw32(oa[2], oa[6]), t3 = sw32(oa[3], oa[7]);
        f32x2 u; u[0] = sw16(t0, t2); u[1] = sw16(t1, t3);
        *(f32x2*)&sO[w][8 * c + 2 * r] = u;
    }
    ST(5);
    bar();
    if (w < 2) {
        float a = 0.f;
#pragma unroll
        for (int k = 0; k < 8; ++k) a += sO[k][tid];
        put(ws + WS_OG + (g * 8 + j) * 128 + tid, thi, tlo, a);
    } else if (w == 2) {
        const f32x4 l0 = *(const f32x4*)&sL[0], l1 = *(const f32x4*)&sL[4];
        const float l = ((l0[0] + l0[1]) + (l0[2] + l0[3])) + ((l1[0] + l1[1]) + (l1[2] + l1[3]));
        if (lane < 2) put(ws + WS_ML + g * 16 + 2 * j + lane, thi, tlo, lane ? l : mx);
    }
    gu64* go = ws + WS_OG + (g * 8 + w) * 128 + lane;
    gu64* gm = ws + WS_ML + g * 16 + c;
    u64 o0 = get(go), o1 = get(go + 64), ml = get(gm);
    __builtin_amdgcn_sched_barrier(0);
    float ya[8];
#pragma unroll
    for (int k = 0; k < 8; ++k) ya[k] = 0.f;
#pragma unroll
    for (int i = 0; i < 2; ++i) {
        const int hr = 6 * i + r6;
        const u32 hl = (ffn && hr < 8) ? sH[8 * w + (hr & 7)] : 0u, hh = hl << 16;
#pragma unroll
        for (int k = 0; k < 4; ++k) { ya[2 * k] = dot2(w2v[i][k], hl, ya[2 * k]); ya[2 * k + 1] = dot2(w2v[i][k], hh, ya[2 * k + 1]); }
    }
    {
        u32 spins = 0;
        if (!__all(okg(o0, thi, tlo) & okg(o1, thi, tlo) & okg(ml, thi, tlo))) {
            asm volatile("; retry o" ::: "memory");
#pragma nounroll
            do {
                if (++spins > kSpinCap) { fail = true; break; }
                o0 = get(go); o1 = get(go + 64); ml = get(gm);
            } while (!__all(okg(o0, thi, tlo) & okg(o1, thi, tlo) & okg(ml, thi, tlo)));
        }
    }
    ST(6);
    {
        const float v = val(ml);
        const float M = row_max((c & 1) ? -INFINITY : v);
        const float mj = dpp<0xA0>(v);
        const float L = row_sum((c & 1) ? v * __expf(mj - M) : 0.f);
        const float mw = __uint_as_float(__builtin_amdgcn_readlane(__float_as_uint(v), 2 * w));
        const float scl = __expf(mw - M) / L;
        sOg[w][lane] = val(o0) * scl; sOg[w][64 + lane] = val(o1) * scl;
    }
    bar();
#pragma unroll
    for (int i = 0; i < 3; ++i) {
        const int row = 16 * w + ((6 * i + r6) & 15);
        float a = sOg[0][row];
#pragma unroll
        for (int k = 1; k < 8; ++k) a += sOg[k][row];
        const u32 ol = (6 * i + r6 < 16) ? bfbits(a) : 0u, oh = ol << 16;
#pragma unroll
        for (int k = 0; k < 4; ++k) { ya[2 * k] = dot2(wov[i][k], ol, ya[2 * k]); ya[2 * k + 1] = dot2(wov[i][k], oh, ya[2 * k + 1]); }
    }
    if (lane < 60) {
        f32x4 a0 = {ya[0], ya[1], ya[2], ya[3]}, a1 = {ya[4], ya[5], ya[6], ya[7]};
        *(f32x4*)&sP[6 * w + r6][8 * cc] = a0; *(f32x4*)&sP[6 * w + r6][8 * cc + 4] = a1;
    }
    bar();
    ST(7);
    if (w < 5) {
        const int col = 16 * w + (lane >> 2), qd = lane & 3;
        float a = 0.f;
#pragma unroll
        for (int k = 0; k < 12; ++k) a += sP[12 * qd + k][col];
        a += dpp<0xB1>(a); a += dpp<0x4E>(a);
        if (fail) a = __builtin_nanf("");
        if (qd == 0) put(ws + WS_Y + ((5 * j + w) * 32 + g) * 16 + (lane >> 2), thi, tlo, a);
    } else if (w == 7 && b < 40) {
        gu64* gy = ws + WS_Y + b * 512 + lane;
        float a; u32 spins = 0;
        for (;;) {
            u64 y[8]; bool ok = true;
#pragma unroll
            for (int i = 0; i < 8; ++i) y[i] = get(gy + 64 * i);
            a = 0.f;
#pragma unroll
            for (int i = 0; i < 8; ++i) { ok &= okg(y[i], thi, tlo); a += val(y[i]); }
            if (__all(ok)) break;
            if (++spins > kSpinCap) { a = __builtin_nanf(""); break; }
        }
        a = col_sum(a);
        if (lane < 16) out[ocol] = (unsigned short)bfbits(lo16(xo) + a + lo16(b2o));
        if (b == 0 && lane == 0) __hip_atomic_store(ws, tg, RLX_AGENT);
    }
    ST(8);
}

extern "C" void kernel_launch(void* const* d_in, const int* in_sizes, int n_in, void* d_out, int out_size,
                              void* d_ws, size_t ws_size, hipStream_t stream) {
    typedef const unsigned short* P;
    fused_decode<<<256, 512, 0, stream>>>((P)d_in[0], (P)d_in[1], (P)d_in[2], (P)d_in[3], (P)d_in[4], (P)d_in[6],
                                          (P)d_in[7], (P)d_in[8], (P)d_in[9], (unsigned short*)d_out, (u64*)d_ws);
}
```

```cpp
#include <hip/hip_runtime.h>
#include <hip/hip_bf16.h>

typedef unsigned u32;
typedef unsigned long long u64;
typedef u32 u32x2 __attribute__((ext_vector_type(2)));
typedef u32 u32x4 __attribute__((ext_vector_type(4)));
typedef float f32x2 __attribute__((ext_vector_type(2)));
typedef float f32x4 __attribute__((ext_vector_type(4)));
typedef __bf16 bf2 __attribute__((ext_vector_type(2)));
typedef __bf16 bf8 __attribute__((ext_vector_type(8)));
typedef __attribute__((address_space(1))) u64 gu64;
typedef __amdgpu_buffer_rsrc_t rsrc_t;

#define RLX_AGENT __ATOMIC_RELAXED, __HIP_MEMORY_SCOPE_AGENT
#define AUX_KV 2
#define AUX_W 2
#define AUX_WO 2
#define GQ_SLEEP 20

constexpr int DM = 640, NH = 32, DH = 128, SK = 8192, WIN = 2048, FF = 1728, NQ = NH * DH;
constexpr u32 CODE_BYTES = 11008;
constexpr int VS = 10;
constexpr u32 OOB = 0x80000000u;
constexpr u32 kSpinCap = 1u << 21;
constexpr int WS_QH = 512;
constexpr int WS_OG = WS_QH + 32 * 8 * 192;
constexpr int WS_ML = WS_OG + 32 * 8 * 128;
constexpr int WS_Y = WS_ML + 32 * 8 * 16;
constexpr int WS_END = WS_Y + 40 * 32 * 16;

#define PACE 12
#define PRIO 0
#define PT0 100
#define PDT 62
#define PI0 0
#define SLOT(n) do { if (PDT && (n) >= PI0) { while ((u32)__builtin_amdgcn_s_memrealtime() - t0 < (u32)(PT0 + ((n) - PI0) * PDT / 4)) { } } } while (0)
#define PACEW(n) do { if (((n) & 1) == 0) SLOT(n); if (PACE && (n) >= PACE) { asm volatile("s_waitcnt vmcnt(%0)" :: "i"(PACE - 1) : "memory"); } __builtin_amdgcn_sched_barrier(0); } while (0)
#define BDELAY 0
#define ML_SLEEP 8
#define O_SLEEP 6
#define Y_SLEEP 8
#define CODEPF 1
#define WS_OFF(sz) 0
#define ST(i) do { } while (0)

__device__ __forceinline__ rsrc_t mk(const void* p, u32 bytes) {
    return __builtin_amdgcn_make_buffer_rsrc((void*)p, 0, bytes, 0x00020000);
}
__device__ __forceinline__ float dot2(u32 a, u32 b, float c) {
    return __builtin_amdgcn_fdot2_f32_bf16(__builtin_bit_cast(bf2, a), __builtin_bit_cast(bf2, b), c, false);
}
__device__ __forceinline__ u32 bfbits(float f) { return (u32)__builtin_bit_cast(unsigned short, (__bf16)f); }
__device__ __forceinline__ float bfr(float f) { return __uint_as_float(bfbits(f) << 16); }
__device__ __forceinline__ float lo16(u32 v) { return __uint_as_float(v << 16); }
__device__ __forceinline__ float hi16(u32 v) { return __uint_as_float(v & 0xffff0000u); }
template <int C> __device__ __forceinline__ float dpp(float v) {
    return __uint_as_float(__builtin_amdgcn_update_dpp(0u, __float_as_uint(v), C, 0xf, 0xf, true));
}
__device__ __forceinline__ float row_sum(float v) {
    v += dpp<0xB1>(v); v += dpp<0x4E>(v); v += dpp<0x141>(v); v += dpp<0x140>(v); return v;
}
__device__ __forceinline__ float row_max(float v) {
    v = fmaxf(v, dpp<0xB1>(v)); v = fmaxf(v, dpp<0x4E>(v)); v = fmaxf(v, dpp<0x141>(v)); v = fmaxf(v, dpp<0x140>(v)); return v;
}
__device__ __forceinline__ float sw32(float a, float b) {
    auto r = __builtin_amdgcn_permlane32_swap(__float_as_uint(a), __float_as_uint(b), false, false);
    return __uint_as_float(r[0]) + __uint_as_float(r[1]);
}
__device__ __forceinline__ float sw16(float a, float b) {
    auto r = __builtin_amdgcn_permlane16_swap(__float_as_uint(a), __float_as_uint(b), false, false);
    return __uint_as_float(r[0]) + __uint_as_float(r[1]);
}
__device__ __forceinline__ float col_sum(float v) { v = sw32(v, v); return sw16(v, v); }
__device__ __forceinline__ float col_max(float v) {
    auto r = __builtin_amdgcn_permlane32_swap(__float_as_uint(v), __float_as_uint(v), false, false);
    v = fmaxf(__uint_as_float(r[0]), __uint_as_float(r[1]));
    auto s = __builtin_amdgcn_permlane16_swap(__float_as_uint(v), __float_as_uint(v), false, false);
    return fmaxf(__uint_as_float(s[0]), __uint_as_float(s[1]));
}
template <int C> __device__ __forceinline__ float rs(float a, float b, bool hi) {
    return (hi ? b : a) + dpp<C>(hi ? a : b);
}
template <int I> __device__ __forceinline__ u32 row_bcast(u32 v) {
    return __builtin_amdgcn_update_dpp(0u, v, 0x150 + I, 0xf, 0xf, true);
}
template <int I, int N> struct MV {
    static __device__ __forceinline__ void run(const u32x4 (&vv)[N], u32 pp, const u32x4& mk4, f32x4& acc) {
        const u32 p = row_bcast<I>(pp);
        const u32x4 a = {p & mk4[0], p & mk4[1], p & mk4[2], p & mk4[3]};
        acc = __builtin_amdgcn_mfma_f32_16x16x32_bf16(__builtin_bit_cast(bf8, a), __builtin_bit_cast(bf8, vv[I]), acc, 0, 0, 0);
        __builtin_amdgcn_sched_barrier(0);
        if constexpr (I + 1 < N) MV<I + 1, N>::run(vv, pp, mk4, acc);
    }
};
__device__ __forceinline__ void bar() {
    asm volatile("s_waitcnt lgkmcnt(0)" ::: "memory");
    __builtin_amdgcn_s_barrier();
    asm volatile("" ::: "memory");
    __builtin_amdgcn_sched_barrier(0);
}
__device__ __forceinline__ void put(gu64* p, u32 thi, u32 tlo, float v) {
    u32 b = ((__float_as_uint(v) + 0x20u) & ~0x3fu) | tlo;
    __hip_atomic_store(p, ((u64)thi << 32) | b, RLX_AGENT);
}
__device__ __forceinline__ u64 get(gu64* p) { return __hip_atomic_load(p, RLX_AGENT); }
__device__ __forceinline__ bool okg(u64 x, u32 thi, u32 tlo) { return ((((u32)(x >> 32)) ^ thi) | (((u32)x & 0x3fu) ^ tlo)) == 0u; }
__device__ __forceinline__ float val(u64 x) { return __uint_as_float((u32)x & ~0x3fu); }

#define EPOCH (*(const volatile __attribute__((address_space(4))) u64*)wsp)

__global__ void __launch_bounds__(512, 2)
fused_decode(const unsigned short* __restrict__ x, const unsigned short* __restrict__ kc,
             const unsigned short* __restrict__ vc, const unsigned short* __restrict__ wq,
             const unsigned short* __restrict__ wo, const unsigned short* __restrict__ w1,
             const unsigned short* __restrict__ b1, const unsigned short* __restrict__ w2,
             const unsigned short* __restrict__ b2, unsigned short* __restrict__ out, u64* wsp) {
    __shared__ float sA[4][192];
    __shared__ float sG[4][192];
    __shared__ float sO[4][128];
    __shared__ float sOg[128][8];
    __shared__ float sS[8];
    __shared__ float sP[8][88];
    __shared__ float sM[4], sL[4];
    __shared__ u32 sH[64];
    __shared__ u32 sCnt, sRdy;
    gu64* ws = (gu64*)wsp;
    const int tid = threadIdx.x, lane = tid & 63;
    const int w = __builtin_amdgcn_readfirstlane(tid >> 6);
    const int b = blockIdx.x, xcd = b & 7, rr = b >> 3, s4 = rr >> 3;
    const int g = xcd * 4 + s4, j = rr & 7;
    const int f = xcd < 5 ? (s4 < 3 ? 3 * xcd + s4 : -1) : 15 + 4 * (xcd - 5) + s4;
    const bool ffn = f >= 0;
    const int r = lane >> 4, c = lane & 15;
    ST(0);
    const u64 code0 = __builtin_amdgcn_s_getpc();
    const u32 t0 = (u32)__builtin_amdgcn_s_memrealtime();
    asm volatile("" :: "s"(x), "s"(kc), "s"(vc), "s"(wq), "s"(wo), "s"(w1), "s"(b1), "s"(w2), "s"(b2), "s"(out), "s"(wsp));
    const rsrc_t rx = mk(x, DM * 2);
    const rsrc_t rwo = mk(wo + (size_t)(g * DH + 16 * w) * DM + 80 * j, 15 * DM * 2 + 160);
    const rsrc_t rw2 = mk(w2 + (size_t)((ffn ? f : 0) * 64 + 8 * w) * DM + 80 * j, ffn ? 7 * DM * 2 + 160 : 0);
    const u32 wofs = c < 10 ? r * DM * 2 + c * 16 : OOB;
    u32x4 wov[4], w2v[2];
    const u32 hm = (c & 1) ? 0xffff0000u : 0x0000ffffu;
    const u32x4 mk4 = {(c >> 1) == 0 ? hm : 0u, (c >> 1) == 1 ? hm : 0u, (c >> 1) == 2 ? hm : 0u, (c >> 1) == 3 ? hm : 0u};
    const int ocol = 16 * b + c;
    u32 xo, b2o, thi, tlo;
    u64 tg;
    bool fail = false;
    gu64* gm = ws + WS_ML + (g * 8 + (w == 3 ? c >> 1 : w)) * 16 + (c & 1);

    if (__builtin_expect(w < 4, 1)) {
        const u32x4 xa = __builtin_amdgcn_raw_buffer_load_b128(rx, lane * 16, 0, 0);
        const u32x4 xb = __builtin_amdgcn_raw_buffer_load_b128(rx, 1024 + lane * 16, 0, 0);
        const int row0 = 80 * j + 20 * w;
        u32 xr[5]; u32x4 wqv[5]; u32x2 w1v[5];
        const rsrc_t rwq = mk(wq + (size_t)row0 * NQ + g * DH, 19 * NQ * 2 + 256);
        const rsrc_t rw1 = mk(w1 + (size_t)row0 * FF + (ffn ? f : 0) * 64, ffn ? 19 * FF * 2 + 128 : 0);
#pragma unroll
        for (int i = 0; i < 5; ++i) xr[i] = __builtin_amdgcn_raw_buffer_load_b16(rx, (row0 + 4 * i + r) * 2, 0, 0);
#pragma unroll
        for (int i = 0; i < 5; ++i) wqv[i] = __builtin_amdgcn_raw_buffer_load_b128(rwq, (4 * i + r) * NQ * 2 + c * 16, 0, AUX_W);
#pragma unroll
        for (int i = 0; i < 5; ++i) w1v[i] = __builtin_amdgcn_raw_buffer_load_b64(rw1, (4 * i + r) * FF * 2 + c * 8, 0, AUX_W);
        const u32 b1v = __builtin_amdgcn_raw_buffer_load_b16(mk(b1 + (ffn ? f : 0) * 64, 128), lane * 2, 0, 0);
        if (tid == 0) { sCnt = 0; sRdy = 0; }
        __builtin_amdgcn_sched_barrier(0);
        bar();
        xo = 0; b2o = 0;
        tg = EPOCH + 1; tlo = (u32)tg & 0x3fu; thi = (u32)(tg >> 6);
        float xs[16];
#pragma unroll
        for (int i = 0; i < 4; ++i) { xs[2 * i] = lo16(xa[i]); xs[2 * i + 1] = hi16(xa[i]); xs[8 + 2 * i] = lo16(xb[i]); xs[9 + 2 * i] = hi16(xb[i]); }
        float sum = 0.f;
#pragma unroll
        for (int i = 0; i < 16; ++i) sum += xs[i];
        const float mean = col_sum(row_sum(sum)) * (1.0f / DM);
        float sq = 0.f, sq2 = 0.f;
#pragma unroll
        for (int i = 0; i < 8; ++i) { float d = xs[i] - mean; sq += d * d; float e = xs[8 + i] - mean; sq2 += e * e; }
        sq += lane < 16 ? sq2 : 0.f;
        const float var = bfr(col_sum(row_sum(sq)) * (1.0f / DM));
        const float mb = bfr(mean), sd = bfr(sqrtf(bfr(var + 1e-5f)));
        ST(1);
        float qa[8], ha[4];
#pragma unroll
        for (int k = 0; k < 8; ++k) qa[k] = 0.f;
#pragma unroll
        for (int k = 0; k < 4; ++k) ha[k] = 0.f;
#pragma unroll
        for (int i = 0; i < 5; ++i) {
            const u32 xl = bfbits(bfr(lo16(xr[i]) - mb) / sd), xh = xl << 16;
#pragma unroll
            for (int k = 0; k < 4; ++k) { qa[2 * k] = dot2(wqv[i][k], xl, qa[2 * k]); qa[2 * k + 1] = dot2(wqv[i][k], xh, qa[2 * k + 1]); }
#pragma unroll
            for (int k = 0; k < 2; ++k) { ha[2 * k] = dot2(w1v[i][k], xl, ha[2 * k]); ha[2 * k + 1] = dot2(w1v[i][k], xh, ha[2 * k + 1]); }
        }
        {
            const float t0 = sw32(qa[0], qa[4]), t1 = sw32(qa[1], qa[5]), t2 = sw32(qa[2], qa[6]), t3 = sw32(qa[3], qa[7]);
            f32x2 u; u[0] = sw16(t0, t2); u[1] = sw16(t1, t3);
            *(f32x2*)&sA[w][8 * c + 2 * r] = u;
            sA[w][128 + 4 * c + r] = sw16(sw32(ha[0], ha[2]), sw32(ha[1], ha[3]));
        }
        asm volatile("s_waitcnt lgkmcnt(0)" ::: "memory");
        u32 arrived = 0;
        if (lane == 0) arrived = __hip_atomic_fetch_add(&sCnt, 1u, __ATOMIC_RELAXED, __HIP_MEMORY_SCOPE_WORKGROUP);
        arrived = __builtin_amdgcn_readfirstlane(arrived);
        asm volatile("" ::: "memory");
        if (arrived == 3) {
#pragma unroll
            for (int k = 0; k < 3; ++k) {
                const int e = 64 * k + lane;
                put(ws + WS_QH + (g * 8 + j) * 192 + e, thi, tlo, (sA[0][e] + sA[1][e]) + (sA[2][e] + sA[3][e]));
            }
        }
        ST(2);
        if (GQ_SLEEP) __builtin_amdgcn_s_sleep(GQ_SLEEP);
        gu64* gq = ws + WS_QH + (g * 8 + 2 * w) * 192 + lane;
        u32 spins = 0;
        u64 q[6];
#pragma unroll
        for (int k = 0; k < 6; ++k) q[k] = get(gq + 64 * k);
        bool ok = true;
#pragma unroll
        for (int k = 0; k < 6; ++k) ok &= okg(q[k], thi, tlo);
        if (!__all(ok)) {
            asm volatile("; retry q" ::: "memory");
#pragma nounroll
            while (!__all(ok)) {
                ++spins;
                if (spins > kSpinCap) { q[0] = 0x7fc00000u; break; }
                ok = true;
#pragma unroll
                for (int k = 0; k < 6; ++k) q[k] = get(gq + 64 * k);
#pragma unroll
                for (int k = 0; k < 6; ++k) ok &= okg(q[k], thi, tlo);
            }
        }
#pragma unroll
        for (int k = 0; k < 3; ++k) sG[w][64 * k + lane] = val(q[k]) + val(q[k + 3]);
        asm volatile("s_waitcnt lgkmcnt(0)" ::: "memory");
        if (lane == 0) __hip_atomic_fetch_add(&sRdy, 1u, __ATOMIC_RELAXED, __HIP_MEMORY_SCOPE_WORKGROUP);
        ST(3);
        bar();
        const bool lateA = (u32)__builtin_amdgcn_s_memrealtime() - t0 >= (u32)(PT0 + (31 - PI0) * PDT / 4);
        if (lateA) {
#pragma unroll
            for (int i = 0; i < 4; ++i) wov[i] = __builtin_amdgcn_raw_buffer_load_b128(rwo, wofs, i * 4 * DM * 2, AUX_WO);
            __builtin_amdgcn_sched_barrier(0);
#pragma unroll
            for (int i = 0; i < 2; ++i) w2v[i] = __builtin_amdgcn_raw_buffer_load_b128(rw2, wofs, i * 4 * DM * 2, AUX_WO);
            __builtin_amdgcn_sched_barrier(0);
        }
        ST(4);
        bar();
        bar();
        if (w == 2) {
            const f32x4 m4 = *(const f32x4*)&sM[0], l4 = *(const f32x4*)&sL[0];
            const float m = fmaxf(fmaxf(m4[0], m4[1]), fmaxf(m4[2], m4[3]));
            if (lane < 2) put(ws + WS_ML + (g * 8 + j) * 16 + lane, thi, tlo, lane ? (l4[0] + l4[1]) + (l4[2] + l4[3]) : m);
        }
        if (w == 0) {
            const float a = (sG[0][128 + lane] + sG[1][128 + lane]) + (sG[2][128 + lane] + sG[3][128 + lane]);
            const float z = bfr(bfr(a) + lo16(b1v));
            const float u = 0.7978845608028654f * (z + 0.044715f * z * z * z);
            const float th = 1.0f - 2.0f / (__expf(2.0f * u) + 1.0f);
            sH[lane] = ffn ? bfbits(0.5f * z * (1.0f + th)) * 0x10001u : 0u;
        }
        if (!lateA) {
            SLOT(32);
#pragma unroll
            for (int i = 0; i < 4; ++i) wov[i] = __builtin_amdgcn_raw_buffer_load_b128(rwo, wofs, i * 4 * DM * 2, AUX_WO);
            __builtin_amdgcn_sched_barrier(0);
            SLOT(35);
#pragma unroll
            for (int i = 0; i < 2; ++i) w2v[i] = __builtin_amdgcn_raw_buffer_load_b128(rw2, wofs, i * 4 * DM * 2, AUX_WO);
            __builtin_amdgcn_sched_barrier(0);
        }
        ST(5);
    } else {
        const size_t kvoff = ((size_t)g * SK + (SK - WIN) + 256 * j) * DH;
        const rsrc_t rk = mk(kc + kvoff, 65536), rv = mk(vc + kvoff, 65536);
        const int rot = (rr * 2 + xcd * 8 + (b & 1) + (w - 4) * 16) * 1024;
        xo = __builtin_amdgcn_raw_buffer_load_b16(rx, ocol * 2, 0, 0);
        b2o = __builtin_amdgcn_raw_buffer_load_b16(mk(b2, DM * 2), ocol * 2, 0, 0);
        u32x4 kv[16], vv[16];
        const u32x4 cpf = __builtin_amdgcn_raw_buffer_load_b128(mk((const void*)(code0 & ~(u64)63), CODEPF ? CODE_BYTES : 0),
                                                                lane * 16 + ((rr * 4 + w) & 15) * 1024, 0, 0);
        tg = EPOCH + 1; tlo = (u32)tg & 0x3fu; thi = (u32)(tg >> 6);
        bar();
        if (BDELAY) __builtin_amdgcn_s_sleep(BDELAY);
#pragma unroll
        for (int i = 0; i < 16; ++i) { PACEW(i); kv[i] = __builtin_amdgcn_raw_buffer_load_b128(rk, lane * 16, (rot + i * 1024) & 0xffff, AUX_KV); }
        __builtin_amdgcn_sched_barrier(0);
        ST(1);
#pragma unroll
        for (int i = 0; i < VS; ++i) { PACEW(16 + i); vv[i] = __builtin_amdgcn_raw_buffer_load_b128(rv, lane * 16, (rot + i * 1024) & 0xffff, AUX_KV); }
        __builtin_amdgcn_sched_barrier(0);
        const bool lateB = __builtin_amdgcn_readfirstlane(__hip_atomic_load(&sRdy, __ATOMIC_RELAXED, __HIP_MEMORY_SCOPE_WORKGROUP)) != 4u;
        if (lateB) {
#pragma unroll
            for (int i = VS; i < 16; ++i) { PACEW(16 + i); vv[i] = __builtin_amdgcn_raw_buffer_load_b128(rv, lane * 16, (rot + i * 1024) & 0xffff, AUX_KV); }
            __builtin_amdgcn_sched_barrier(0);
        }
        ST(2);
        asm volatile("" :: "v"(cpf));
        ST(3);
        bar();
        u32 qb[4];
        {
            const int e = 8 * c + 2 * r;
            const f32x2 a = (*(const f32x2*)&sG[0][e] + *(const f32x2*)&sG[1][e]) + (*(const f32x2*)&sG[2][e] + *(const f32x2*)&sG[3][e]);
            const u32 pk = bfbits(a[0]) | (bfbits(a[1]) << 16);
            auto s = __builtin_amdgcn_permlane32_swap(pk, pk, false, false);
            auto s0 = __builtin_amdgcn_permlane16_swap(s[0], s[0], false, false);
            auto s1 = __builtin_amdgcn_permlane16_swap(s[1], s[1], false, false);
            qb[0] = s0[0]; qb[1] = s0[1]; qb[2] = s1[0]; qb[3] = s1[1];
        }
        float sc[16];
#pragma unroll
        for (int i = 0; i < 16; ++i) {
            float a = dot2(kv[i][0], qb[0], 0.f);
            a = dot2(kv[i][1], qb[1], a); a = dot2(kv[i][2], qb[2], a); sc[i] = dot2(kv[i][3], qb[3], a);
        }
        if (!lateB) {
#pragma unroll
            for (int i = VS; i < VS + 2; ++i) { PACEW(16 + i); vv[i] = __builtin_amdgcn_raw_buffer_load_b128(rv, lane * 16, (rot + i * 1024) & 0xffff, AUX_KV); }
        }
        __builtin_amdgcn_sched_barrier(0);
#pragma unroll
        for (int k = 0; k < 8; ++k) sc[k] = rs<0x140>(sc[k], sc[k + 8], c & 8);
#pragma unroll
        for (int k = 0; k < 4; ++k) sc[k] = rs<0x141>(sc[k], sc[k + 4], c & 4);
#pragma unroll
        for (int k = 0; k < 2; ++k) sc[k] = rs<0x4E>(sc[k], sc[k + 2], c & 2);
        const float s1 = bfr(bfr(rs<0xB1>(sc[0], sc[1], c & 1)) * 0.08838834764831845f);
        float mx = col_max(row_max(s1));
        if (lane == 0) sM[w - 4] = mx;
        if (!lateB) {
#pragma unroll
            for (int i = VS + 2; i < VS + 4; ++i) { PACEW(16 + i); vv[i] = __builtin_amdgcn_raw_buffer_load_b128(rv, lane * 16, (rot + i * 1024) & 0xffff, AUX_KV); }
        }
        __builtin_amdgcn_sched_barrier(0);
        bar();
        {
            const f32x4 m4 = *(const f32x4*)&sM[0];
            mx = fmaxf(fmaxf(m4[0], m4[1]), fmaxf(m4[2], m4[3]));
        }
        const float pf = __expf(s1 - mx);
        const u32 pp = bfbits(pf) * 0x10001u;
        const float ls = col_sum(row_sum(pf));
        if (lane == 0) sL[w - 4] = ls;
        bar();
        ST(4);
        if (!lateB) {
#pragma unroll
            for (int i = VS + 4; i < 16; ++i) { PACEW(16 + i); vv[i] = __builtin_amdgcn_raw_buffer_load_b128(rv, lane * 16, (rot + i * 1024) & 0xffff, AUX_KV); }
        }
        __builtin_amdgcn_sched_barrier(0);
        {
            SLOT(33);
#pragma unroll
            for (int i = 0; i < 4; ++i) wov[i] = __builtin_amdgcn_raw_buffer_load_b128(rwo, wofs, i * 4 * DM * 2, AUX_WO);
            __builtin_amdgcn_sched_barrier(0);
            SLOT(36);
#pragma unroll
            for (int i = 0; i < 2; ++i) w2v[i] = __builtin_amdgcn_raw_buffer_load_b128(rw2, wofs, i * 4 * DM * 2, AUX_WO);
            __builtin_amdgcn_sched_barrier(0);
        }
        f32x4 acc = {0.f, 0.f, 0.f, 0.f};
        MV<0, 16>::run(vv, pp, mk4, acc);
        if (lane < 32) *(f32x4*)&sO[w - 4][8 * c + 4 * r] = acc;
        ST(5);
    }
    bar();
    if (w < 2) put(ws + WS_OG + (g * 8 + j) * 128 + tid, thi, tlo, (sO[0][tid] + sO[1][tid]) + (sO[2][tid] + sO[3][tid]));
    if (O_SLEEP) __builtin_amdgcn_s_sleep(O_SLEEP);
    gu64* go = ws + WS_OG + (g * 8 + w) * 128 + lane;
    u64 ml = get(gm);
    u64 o0 = get(go), o1 = get(go + 64);
    __builtin_amdgcn_sched_barrier(0);
    f32x4 ya = {0.f, 0.f, 0.f, 0.f};
    MV<0, 2>::run(w2v, sH[8 * w + 4 * (c & 1) + r], mk4, ya);
    if (w == 3) {
        u32 spins = 0;
        if (!__all(okg(ml, thi, tlo))) {
            asm volatile("; retry ml" ::: "memory");
#pragma nounroll
            do {
                if (++spins > kSpinCap) { fail = true; break; }
                ml = get(gm);
            } while (!__all(okg(ml, thi, tlo)));
        }
        const float v = val(ml);
        const float M = row_max((c & 1) ? -INFINITY : v);
        const float e = __expf(dpp<0xA0>(v) - M);
        const float L = row_sum((c & 1) ? v * e : 0.f);
        if (lane < 16 && !(c & 1)) sS[c >> 1] = fail ? __builtin_nanf("") : e / L;
    }
    __builtin_amdgcn_sched_barrier(0);
    {
        u32 spins = 0;
        if (!__all(okg(o0, thi, tlo) & okg(o1, thi, tlo))) {
            asm volatile("; retry o" ::: "memory");
#pragma nounroll
            do {
                if (++spins > kSpinCap) { fail = true; break; }
                o0 = get(go); o1 = get(go + 64);
            } while (!__all(okg(o0, thi, tlo) & okg(o1, thi, tlo)));
        }
    }
    sOg[lane][w] = fail ? __builtin_nanf("") : val(o0); sOg[64 + lane][w] = val(o1);
    ST(6);
    bar();
    {
        const f32x2 ov = *(const f32x2*)&sOg[16 * w + 4 * (c & 3) + r][2 * (c >> 2)], sv = *(const f32x2*)&sS[2 * (c >> 2)];
        float a = ov[0] * sv[0] + ov[1] * sv[1];
        a += dpp<0x124>(a); a += dpp<0x128>(a);
        MV<0, 4>::run(wov, bfbits(a) * 0x10001u, mk4, ya);
    }
    if (lane < 32 && c < 10) *(f32x4*)&sP[w][8 * c + 4 * r] = ya;
    bar();
    ST(7);
    if (w < 5) {
        const int col = 16 * w + (lane >> 2), qd = lane & 3;
        float a = sP[2 * qd][col] + sP[2 * qd + 1][col];
        a += dpp<0xB1>(a); a += dpp<0x4E>(a);
        if (qd == 0) put(ws + WS_Y + ((5 * j + w) * 32 + g) * 16 + (lane >> 2), thi, tlo, a);
    } else if (w == 7 && b < 40) {
        gu64* gy = ws + WS_Y + b * 512 + lane;
        if (Y_SLEEP) __builtin_amdgcn_s_sleep(Y_SLEEP);
        float a; u32 spins = 0;
        for (;;) {
            u64 y[8]; bool ok = true;
#pragma unroll
            for (int i = 0; i < 8; ++i) y[i] = get(gy + 64 * i);
            a = 0.f;
#pragma unroll
            for (int i = 0; i < 8; ++i) { ok &= okg(y[i], thi, tlo); a += val(y[i]); }
            if (__all(ok)) break;
            if (++spins > kSpinCap) { a = __builtin_nanf(""); break; }
        }
        a = col_sum(a);
        if (lane < 16) out[ocol] = (unsigned short)bfbits(lo16(xo) + a + lo16(b2o));
        if (b == 0 && lane == 0) __hip_atomic_store(ws, tg, RLX_AGENT);
    }
    ST(8);
}

extern "C" void kernel_launch(void* const* d_in, const int* in_sizes, int n_in, void* d_out, int out_size,
                              void* d_ws, size_t ws_size, hipStream_t stream) {
    typedef const unsigned short* P;
    fused_decode<<<256, 512, 0, stream>>>((P)d_in[0], (P)d_in[1], (P)d_in[2], (P)d_in[3], (P)d_in[4], (P)d_in[6],
                                          (P)d_in[7], (P)d_in[8], (P)d_in[9], (unsigned short*)d_out,
                                          (u64*)((char*)d_ws + WS_OFF(ws_size)));
}
```

```cpp
#include <hip/hip_runtime.h>
#include <hip/hip_bf16.h>

typedef unsigned u32;
typedef unsigned long long u64;
typedef u32 u32x2 __attribute__((ext_vector_type(2)));
typedef u32 u32x4 __attribute__((ext_vector_type(4)));
typedef float f32x2 __attribute__((ext_vector_type(2)));
typedef float f32x4 __attribute__((ext_vector_type(4)));
typedef __bf16 bf2 __attribute__((ext_vector_type(2)));
typedef __bf16 bf8 __attribute__((ext_vector_type(8)));
typedef short s16x4 __attribute__((ext_vector_type(4)));
typedef __attribute__((address_space(1))) u64 gu64;
typedef __amdgpu_buffer_rsrc_t rsrc_t;

#define RLX_AGENT __ATOMIC_RELAXED, __HIP_MEMORY_SCOPE_AGENT
#define AUX_KV 2
#define AUX_W 2
#define AUX_WO 2
#define GQ_SLEEP 10

constexpr int DM = 640, NH = 32, DH = 128, SK = 8192, WIN = 2048, FF = 1728, NQ = NH * DH;
constexpr u32 CODE_BYTES = 10240;
#define VSN 10
constexpr int VS = VSN;
constexpr u32 OOB = 0x80000000u;
constexpr u32 kSpinCap = 1u << 21;
constexpr int WS_QH = 512;
constexpr int WS_OG = WS_QH + 32 * 8 * 192;
constexpr int WS_ML = WS_OG + 32 * 8 * 128;
constexpr int WS_Y = WS_ML + 32 * 8 * 16;
constexpr int WS_T = WS_Y + 640;
constexpr int WS_END = WS_T + 32 * 16;

#define PACE 14
#define PT0 120
#define PDT 58
constexpr u32 T16 = PT0 + 16 * PDT / 4;
#define SLOT_T(n) ((n) < 16 ? (u32)(PT0 + (n) * PDT / 4) : T16 + ((u32)((n) - 16) * pdt2 >> 2))
#define SLOT(n) do { const u32 tt_ = SLOT_T(n); while ((u32)__builtin_amdgcn_s_memrealtime() - t0 < tt_) { } } while (0)
#define PACEW(n) do { if (((n) & 1) == 0) SLOT(n); if (PACE && (n) >= PACE) { asm volatile("s_waitcnt vmcnt(%0)" :: "i"(PACE - 1) : "memory"); } __builtin_amdgcn_sched_barrier(0); } while (0)
#define O_SLEEP 8
#define CODEPF 1
#define TEND 690
#define GQT 335
#define ORGI 7
#define WS_OFF(sz) 0
#define ST(i) do { } while (0)

__device__ __forceinline__ rsrc_t mk(const void* p, u32 bytes) {
    return __builtin_amdgcn_make_buffer_rsrc((void*)p, 0, bytes, 0x00020000);
}
__device__ __forceinline__ float dot2(u32 a, u32 b, float c) {
    return __builtin_amdgcn_fdot2_f32_bf16(__builtin_bit_cast(bf2, a), __builtin_bit_cast(bf2, b), c, false);
}
__device__ __forceinline__ u32 bfbits(float f) { return (u32)__builtin_bit_cast(unsigned short, (__bf16)f); }
__device__ __forceinline__ float bfr(float f) { return __uint_as_float(bfbits(f) << 16); }
__device__ __forceinline__ float lo16(u32 v) { return __uint_as_float(v << 16); }
__device__ __forceinline__ float hi16(u32 v) { return __uint_as_float(v & 0xffff0000u); }
template <int C> __device__ __forceinline__ float dpp(float v) {
    return __uint_as_float(__builtin_amdgcn_update_dpp(0u, __float_as_uint(v), C, 0xf, 0xf, true));
}
__device__ __forceinline__ float row_sum(float v) {
    v += dpp<0xB1>(v); v += dpp<0x4E>(v); v += dpp<0x141>(v); v += dpp<0x140>(v); return v;
}
__device__ __forceinline__ float row_max(float v) {
    v = fmaxf(v, dpp<0xB1>(v)); v = fmaxf(v, dpp<0x4E>(v)); v = fmaxf(v, dpp<0x141>(v)); v = fmaxf(v, dpp<0x140>(v)); return v;
}
__device__ __forceinline__ float sw32(float a, float b) {
    auto r = __builtin_amdgcn_permlane32_swap(__float_as_uint(a), __float_as_uint(b), false, false);
    return __uint_as_float(r[0]) + __uint_as_float(r[1]);
}
__device__ __forceinline__ float sw16(float a, float b) {
    auto r = __builtin_amdgcn_permlane16_swap(__float_as_uint(a), __float_as_uint(b), false, false);
    return __uint_as_float(r[0]) + __uint_as_float(r[1]);
}
__device__ __forceinline__ float col_sum(float v) { v = sw32(v, v); return sw16(v, v); }
__device__ __forceinline__ float col_max(float v) {
    auto r = __builtin_amdgcn_permlane32_swap(__float_as_uint(v), __float_as_uint(v), false, false);
    v = fmaxf(__uint_as_float(r[0]), __uint_as_float(r[1]));
    auto s = __builtin_amdgcn_permlane16_swap(__float_as_uint(v), __float_as_uint(v), false, false);
    return fmaxf(__uint_as_float(s[0]), __uint_as_float(s[1]));
}
template <int C> __device__ __forceinline__ float rs(float a, float b, bool hi) {
    return (hi ? b : a) + dpp<C>(hi ? a : b);
}
template <int I> __device__ __forceinline__ u32 row_bcast(u32 v) {
    return __builtin_amdgcn_update_dpp(0u, v, 0x150 + I, 0xf, 0xf, true);
}
template <int I, int N> struct MV {
    static __device__ __forceinline__ void run(const u32x4 (&vv)[N], u32 pp, const u32x4& mk4, f32x4& acc) {
        const u32 p = row_bcast<I>(pp);
        const u32x4 a = {p & mk4[0], p & mk4[1], p & mk4[2], p & mk4[3]};
        acc = __builtin_amdgcn_mfma_f32_16x16x32_bf16(__builtin_bit_cast(bf8, a), __builtin_bit_cast(bf8, vv[I]), acc, 0, 0, 0);
        __builtin_amdgcn_sched_barrier(0);
        if constexpr (I + 1 < N) MV<I + 1, N>::run(vv, pp, mk4, acc);
    }
};
__device__ __forceinline__ void bar() {
    asm volatile("s_waitcnt lgkmcnt(0)" ::: "memory");
    __builtin_amdgcn_s_barrier();
    asm volatile("" ::: "memory");
    __builtin_amdgcn_sched_barrier(0);
}
__device__ __forceinline__ void put(gu64* p, u32 thi, u32 tlo, float v) {
    u32 b = ((__float_as_uint(v) + 0x20u) & ~0x3fu) | tlo;
    __hip_atomic_store(p, ((u64)thi << 32) | b, RLX_AGENT);
}
__device__ __forceinline__ u64 get(gu64* p) { return __hip_atomic_load(p, RLX_AGENT); }
__device__ __forceinline__ bool okg(u64 x, u32 thi, u32 tlo) { return ((((u32)(x >> 32)) ^ thi) | (((u32)x & 0x3fu) ^ tlo)) == 0u; }
__device__ __forceinline__ float val(u64 x) { return __uint_as_float((u32)x & ~0x3fu); }

#define EPOCH (*(const volatile __attribute__((address_space(4))) u64*)wsp)

__global__ void __launch_bounds__(512, 2)
fused_decode(const unsigned short* __restrict__ x, const unsigned short* __restrict__ kc,
             const unsigned short* __restrict__ vc, const unsigned short* __restrict__ wq,
             const unsigned short* __restrict__ wo, const unsigned short* __restrict__ w1,
             const unsigned short* __restrict__ b1, const unsigned short* __restrict__ w2,
             const unsigned short* __restrict__ b2, unsigned short* __restrict__ out, u64* wsp) {
    __shared__ float sA[4][192];
    __shared__ float sG[4][192];
    __shared__ float sO[4][128];
    __shared__ float sOg[128][8];
    __shared__ float sS[8];
    __shared__ float sP[8][88];
    __shared__ float sM[4], sL[4];
    __shared__ u32 sH[64];
    __shared__ u32 sCnt, sRdy, sDt, sOk;
    gu64* ws = (gu64*)wsp;
    const int tid = threadIdx.x, lane = tid & 63;
    const int w = __builtin_amdgcn_readfirstlane(tid >> 6);
    const int b = blockIdx.x, xcd = b & 7, rr = b >> 3, s4 = rr >> 3;
    const int g = xcd * 4 + s4, j = rr & 7;
    const int f = xcd < 5 ? (s4 < 3 ? 3 * xcd + s4 : -1) : 15 + 4 * (xcd - 5) + s4;
    const bool ffn = f >= 0;
    const int r = lane >> 4, c = lane & 15;
    ST(0);
    const u64 code0 = __builtin_amdgcn_s_getpc();
    const u32 t0 = (u32)__builtin_amdgcn_s_memrealtime();
    asm volatile("" :: "s"(x), "s"(kc), "s"(vc), "s"(wq), "s"(wo), "s"(w1), "s"(b1), "s"(w2), "s"(b2), "s"(out), "s"(wsp));
    const rsrc_t rx = mk(x, DM * 2);
    const rsrc_t rwo = mk(wo + (size_t)(g * DH + 16 * w) * DM + 80 * j, 15 * DM * 2 + 160);
    const rsrc_t rw2 = mk(w2 + (size_t)((ffn ? f : 0) * 64 + 8 * w) * DM + 80 * j, ffn ? 7 * DM * 2 + 160 : 0);
    const u32 wofs = c < 10 ? r * DM * 2 + c * 16 : OOB;
    u32x4 wov[4], w2v[2];
    const u32 hm = (c & 1) ? 0xffff0000u : 0x0000ffffu;
    const u32x4 mk4 = {(c >> 1) == 0 ? hm : 0u, (c >> 1) == 1 ? hm : 0u, (c >> 1) == 2 ? hm : 0u, (c >> 1) == 3 ? hm : 0u};
    const int ocol = 80 * j + 16 * w + (lane >> 2);
    u32 xo, b2o, thi, tlo;
    u64 tg;
    bool fail = false;
    u32 pdt2 = PDT;
    gu64* gm = ws + WS_ML + (g * 8 + (w == 3 ? c >> 1 : w)) * 16 + (c & 1);

    if (__builtin_expect(w < 4, 1)) {
        const u32x4 xa = __builtin_amdgcn_raw_buffer_load_b128(rx, lane * 16, 0, 0);
        const u32x4 xb = __builtin_amdgcn_raw_buffer_load_b128(rx, 1024 + lane * 16, 0, 0);
        const int row0 = 80 * j + 20 * w;
        u32 xr[5]; u32x4 wqv[5]; u32x2 w1v[5];
        const rsrc_t rwq = mk(wq + (size_t)row0 * NQ + g * DH, 19 * NQ * 2 + 256);
        const rsrc_t rw1 = mk(w1 + (size_t)row0 * FF + (ffn ? f : 0) * 64, ffn ? 19 * FF * 2 + 128 : 0);
#pragma unroll
        for (int i = 0; i < 5; ++i) xr[i] = __builtin_amdgcn_raw_buffer_load_b16(rx, (row0 + 4 * i + r) * 2, 0, 0);
#pragma unroll
        for (int i = 0; i < 5; ++i) wqv[i] = __builtin_amdgcn_raw_buffer_load_b128(rwq, (4 * i + r) * NQ * 2 + c * 16, 0, AUX_W);
#pragma unroll
        for (int i = 0; i < 5; ++i) w1v[i] = __builtin_amdgcn_raw_buffer_load_b64(rw1, (4 * i + r) * FF * 2 + c * 8, 0, AUX_W);
        const u32 b1v = __builtin_amdgcn_raw_buffer_load_b16(mk(b1 + (ffn ? f : 0) * 64, 128), lane * 2, 0, 0);
        xo = __builtin_amdgcn_raw_buffer_load_b16(rx, ocol * 2, 0, 0);
        b2o = __builtin_amdgcn_raw_buffer_load_b16(mk(b2, DM * 2), ocol * 2, 0, 0);
        if (tid == 0) { sCnt = 0; sRdy = 0; }
        __builtin_amdgcn_sched_barrier(0);
        bar();
        tg = EPOCH + 1; tlo = (u32)tg & 0x3fu; thi = (u32)(tg >> 6);
        float xs[16];
#pragma unroll
        for (int i = 0; i < 4; ++i) { xs[2 * i] = lo16(xa[i]); xs[2 * i + 1] = hi16(xa[i]); xs[8 + 2 * i] = lo16(xb[i]); xs[9 + 2 * i] = hi16(xb[i]); }
        float sum = 0.f;
#pragma unroll
        for (int i = 0; i < 16; ++i) sum += xs[i];
        const float mean = col_sum(row_sum(sum)) * (1.0f / DM);
        float sq = 0.f, sq2 = 0.f;
#pragma unroll
        for (int i = 0; i < 8; ++i) { float d = xs[i] - mean; sq += d * d; float e = xs[8 + i] - mean; sq2 += e * e; }
        sq += lane < 16 ? sq2 : 0.f;
        const float var = bfr(col_sum(row_sum(sq)) * (1.0f / DM));
        const float mb = bfr(mean), sd = bfr(sqrtf(bfr(var + 1e-5f)));
        ST(1);
        u32x4 xm[5];
#pragma unroll
        for (int i = 0; i < 5; ++i) {
            const u32 xx = bfbits(bfr(lo16(xr[i]) - mb) / sd) * 0x10001u;
            xm[i] = u32x4{xx & mk4[0], xx & mk4[1], xx & mk4[2], xx & mk4[3]};
        }
        __builtin_amdgcn_sched_barrier(0);
        f32x4 qa = {0.f, 0.f, 0.f, 0.f}, ha = {0.f, 0.f, 0.f, 0.f};
#pragma unroll
        for (int i = 0; i < 5; ++i)
            qa = __builtin_amdgcn_mfma_f32_16x16x32_bf16(__builtin_bit_cast(bf8, xm[i]), __builtin_bit_cast(bf8, wqv[i]), qa, 0, 0, 0);
#pragma unroll
        for (int i = 0; i < 5; ++i)
            ha = __builtin_amdgcn_mfma_f32_16x16x16bf16_1k(__builtin_bit_cast(s16x4, u32x2{xm[i][0], xm[i][1]}), __builtin_bit_cast(s16x4, w1v[i]), ha, 0, 0, 0);
        if (lane < 32) *(f32x4*)&sA[w][8 * c + 4 * r] = qa;
        if (lane < 16) *(f32x4*)&sA[w][128 + 4 * c] = ha;
        asm volatile("s_waitcnt lgkmcnt(0)" ::: "memory");
        u32 arrived = 0;
        if (lane == 0) arrived = __hip_atomic_fetch_add(&sCnt, 1u, __ATOMIC_RELAXED, __HIP_MEMORY_SCOPE_WORKGROUP);
        arrived = __builtin_amdgcn_readfirstlane(arrived);
        asm volatile("" ::: "memory");
        if (arrived == 3) {
#pragma unroll
            for (int k = 0; k < 3; ++k) {
                const int e = 64 * k + lane;
                put(ws + WS_QH + (g * 8 + j) * 192 + e, thi, tlo, (sA[0][e] + sA[1][e]) + (sA[2][e] + sA[3][e]));
            }
        }
        ST(2);
        __builtin_amdgcn_s_sleep(GQ_SLEEP);
        while ((u32)__builtin_amdgcn_s_memrealtime() - t0 < (u32)GQT) { }
        gu64* gq = ws + WS_QH + (g * 8 + 2 * w) * 192 + lane;
        u32 spins = 0;
        u64 q[6];
#pragma unroll
        for (int k = 0; k < 6; ++k) q[k] = get(gq + 64 * k);
        bool ok = true;
#pragma unroll
        for (int k = 0; k < 6; ++k) ok &= okg(q[k], thi, tlo);
        if (!__all(ok)) {
            asm volatile("; retry q" ::: "memory");
#pragma nounroll
            while (!__all(ok)) {
                ++spins;
                if (spins > kSpinCap) { q[0] = 0x7fc00000u; break; }
                ok = true;
#pragma unroll
                for (int k = 0; k < 6; ++k) q[k] = get(gq + 64 * k);
#pragma unroll
                for (int k = 0; k < 6; ++k) ok &= okg(q[k], thi, tlo);
            }
        }
#pragma unroll
        for (int k = 0; k < 3; ++k) sG[w][64 * k + lane] = val(q[k]) + val(q[k + 3]);
        asm volatile("s_waitcnt lgkmcnt(0)" ::: "memory");
        if (lane == 0) __hip_atomic_fetch_add(&sRdy, 1u, __ATOMIC_RELAXED, __HIP_MEMORY_SCOPE_WORKGROUP);
        ST(3);
        bar();
        pdt2 = __builtin_amdgcn_readfirstlane(sDt);
        const bool lateA = (u32)__builtin_amdgcn_s_memrealtime() - t0 >= SLOT_T(31);
        if (lateA) {
#pragma unroll
            for (int i = 0; i < 4; ++i) wov[i] = __builtin_amdgcn_raw_buffer_load_b128(rwo, wofs, i * 4 * DM * 2, AUX_WO);
            __builtin_amdgcn_sched_barrier(0);
#pragma unroll
            for (int i = 0; i < 2; ++i) w2v[i] = __builtin_amdgcn_raw_buffer_load_b128(rw2, wofs, i * 4 * DM * 2, AUX_WO);
            __builtin_amdgcn_sched_barrier(0);
        }
        ST(4);
        bar();
        bar();
        if (w == 2) {
            const f32x4 m4 = *(const f32x4*)&sM[0], l4 = *(const f32x4*)&sL[0];
            const float m = fmaxf(fmaxf(m4[0], m4[1]), fmaxf(m4[2], m4[3]));
            if (lane < 2) put(ws + WS_ML + (g * 8 + j) * 16 + lane, thi, tlo, lane ? (l4[0] + l4[1]) + (l4[2] + l4[3]) : m);
        }
        if (w == 0) {
            const float a = (sG[0][128 + lane] + sG[1][128 + lane]) + (sG[2][128 + lane] + sG[3][128 + lane]);
            const float z = bfr(bfr(a) + lo16(b1v));
            const float u = 0.7978845608028654f * (z + 0.044715f * z * z * z);
            const float th = 1.0f - 2.0f / (__expf(2.0f * u) + 1.0f);
            sH[lane] = ffn ? bfbits(0.5f * z * (1.0f + th)) * 0x10001u : 0u;
        }
        if (!lateA) {
            SLOT(32);
#pragma unroll
            for (int i = 0; i < 4; ++i) wov[i] = __builtin_amdgcn_raw_buffer_load_b128(rwo, wofs, i * 4 * DM * 2, AUX_WO);
            __builtin_amdgcn_sched_barrier(0);
            SLOT(35);
#pragma unroll
            for (int i = 0; i < 2; ++i) w2v[i] = __builtin_amdgcn_raw_buffer_load_b128(rw2, wofs, i * 4 * DM * 2, AUX_WO);
            __builtin_amdgcn_sched_barrier(0);
        }
        ST(5);
    } else {
        const size_t kvoff = ((size_t)g * SK + (SK - WIN) + 256 * j) * DH;
        const rsrc_t rk = mk(kc + kvoff, 65536), rv = mk(vc + kvoff, 65536);
        const int rot = (rr * 2 + xcd * 8 + (b & 1) + (w - 4) * 16) * 1024;
        xo = __builtin_amdgcn_raw_buffer_load_b16(rx, ocol * 2, 0, 0);
        b2o = __builtin_amdgcn_raw_buffer_load_b16(mk(b2, DM * 2), ocol * 2, 0, 0);
        u32x4 kv[16], vv[16];
        const u32x4 cpf = __builtin_amdgcn_raw_buffer_load_b128(mk((const void*)(code0 & ~(u64)63), CODEPF ? CODE_BYTES : 0),
                                                                lane * 16 + ((rr * 4 + w) & 15) * 1024, 0, 0);
        tg = EPOCH + 1; tlo = (u32)tg & 0x3fu; thi = (u32)(tg >> 6);
        bar();
        if (b == 0 && w == 4) {
#pragma unroll
            for (int k = 0; k < 10; ++k) __hip_atomic_store(ws + WS_Y + 64 * k + lane, 0ull, RLX_AGENT);
            asm volatile("s_waitcnt vmcnt(0)" ::: "memory");
            if (lane < 32) __hip_atomic_store(ws + WS_T + lane * 16, ((u64)thi << 32) | (t0 << 6) | tlo, RLX_AGENT);
        }
        u64 org = 0;
#pragma unroll
        for (int i = 0; i < 16; ++i) {
            PACEW(i); kv[i] = __builtin_amdgcn_raw_buffer_load_b128(rk, lane * 16, (rot + i * 1024) & 0xffff, AUX_KV);
            if (i == ORGI) org = get(ws + WS_T + g * 16);
        }
        __builtin_amdgcn_sched_barrier(0);
        {
            const u32 ohi = __builtin_amdgcn_readfirstlane((u32)(org >> 32)), olo = __builtin_amdgcn_readfirstlane((u32)org);
            int d = (int)((t0 << 6) - (olo & ~0x3fu)) >> 6;
            const bool seen = ohi == thi && (olo & 0x3fu) == tlo;
            d = seen ? (d < 0 ? 0 : d > 150 ? 150 : d) : 0;
            pdt2 = (u32)((int)(TEND - T16) - d) / 5u;
            if (tid == 256) { sDt = pdt2; sOk = seen; }
        }
        ST(1);
#pragma unroll
        for (int i = 0; i < VS; ++i) { PACEW(16 + i); vv[i] = __builtin_amdgcn_raw_buffer_load_b128(rv, lane * 16, (rot + i * 1024) & 0xffff, AUX_KV); }
        __builtin_amdgcn_sched_barrier(0);
        const bool lateB = __builtin_amdgcn_readfirstlane(__hip_atomic_load(&sRdy, __ATOMIC_RELAXED, __HIP_MEMORY_SCOPE_WORKGROUP)) != 4u;
        if (lateB) {
#pragma unroll
            for (int i = VS; i < 16; ++i) { PACEW(16 + i); vv[i] = __builtin_amdgcn_raw_buffer_load_b128(rv, lane * 16, (rot + i * 1024) & 0xffff, AUX_KV); }
            __builtin_amdgcn_sched_barrier(0);
        }
        ST(2);
        asm volatile("" :: "v"(cpf));
        ST(3);
        bar();
        u32 qb[4];
        {
            const int e = 8 * c + 2 * r;
            const f32x2 a = (*(const f32x2*)&sG[0][e] + *(const f32x2*)&sG[1][e]) + (*(const f32x2*)&sG[2][e] + *(const f32x2*)&sG[3][e]);
            const u32 pk = bfbits(a[0]) | (bfbits(a[1]) << 16);
            auto s = __builtin_amdgcn_permlane32_swap(pk, pk, false, false);
            auto s0 = __builtin_amdgcn_permlane16_swap(s[0], s[0], false, false);
            auto s1 = __builtin_amdgcn_permlane16_swap(s[1], s[1], false, false);
            qb[0] = s0[0]; qb[1] = s0[1]; qb[2] = s1[0]; qb[3] = s1[1];
        }
        float sc[16];
#pragma unroll
        for (int i = 0; i < 16; ++i) {
            float a = dot2(kv[i][0], qb[0], 0.f);
            a = dot2(kv[i][1], qb[1], a); a = dot2(kv[i][2], qb[2], a); sc[i] = dot2(kv[i][3], qb[3], a);
        }
        if (!lateB) {
#pragma unroll
            for (int i = VS; i < VS + 2; ++i) { PACEW(16 + i); vv[i] = __builtin_amdgcn_raw_buffer_load_b128(rv, lane * 16, (rot + i * 1024) & 0xffff, AUX_KV); }
        }
        __builtin_amdgcn_sched_barrier(0);
#pragma unroll
        for (int k = 0; k < 8; ++k) sc[k] = rs<0x140>(sc[k], sc[k + 8], c & 8);
#pragma unroll
        for (int k = 0; k < 4; ++k) sc[k] = rs<0x141>(sc[k], sc[k + 4], c & 4);
#pragma unroll
        for (int k = 0; k < 2; ++k) sc[k] = rs<0x4E>(sc[k], sc[k + 2], c & 2);
        const float s1 = bfr(bfr(rs<0xB1>(sc[0], sc[1], c & 1)) * 0.08838834764831845f);
        float mx = col_max(row_max(s1));
        if (lane == 0) sM[w - 4] = mx;
        if (!lateB) {
#pragma unroll
            for (int i = VS + 2; i < VS + 4; ++i) { PACEW(16 + i); vv[i] = __builtin_amdgcn_raw_buffer_load_b128(rv, lane * 16, (rot + i * 1024) & 0xffff, AUX_KV); }
        }
        __builtin_amdgcn_sched_barrier(0);
        bar();
        {
            const f32x4 m4 = *(const f32x4*)&sM[0];
            mx = fmaxf(fmaxf(m4[0], m4[1]), fmaxf(m4[2], m4[3]));
        }
        const float pf = __expf(s1 - mx);
        const u32 pp = bfbits(pf) * 0x10001u;
        const float ls = col_sum(row_sum(pf));
        if (lane == 0) sL[w - 4] = ls;
        bar();
        ST(4);
        if (!lateB) {
#pragma unroll
            for (int i = VS + 4; i < 16; ++i) { PACEW(16 + i); vv[i] = __builtin_amdgcn_raw_buffer_load_b128(rv, lane * 16, (rot + i * 1024) & 0xffff, AUX_KV); }
        }
        __builtin_amdgcn_sched_barrier(0);
        {
            SLOT(33);
#pragma unroll
            for (int i = 0; i < 4; ++i) wov[i] = __builtin_amdgcn_raw_buffer_load_b128(rwo, wofs, i * 4 * DM * 2, AUX_WO);
            __builtin_amdgcn_sched_barrier(0);
            SLOT(36);
#pragma unroll
            for (int i = 0; i < 2; ++i) w2v[i] = __builtin_amdgcn_raw_buffer_load_b128(rw2, wofs, i * 4 * DM * 2, AUX_WO);
            __builtin_amdgcn_sched_barrier(0);
        }
        f32x4 acc = {0.f, 0.f, 0.f, 0.f};
        MV<0, 16>::run(vv, pp, mk4, acc);
        if (lane < 32) *(f32x4*)&sO[w - 4][8 * c + 4 * r] = acc;
        ST(5);
    }
    bar();
    if (w < 2) put(ws + WS_OG + (g * 8 + j) * 128 + tid, thi, tlo, (sO[0][tid] + sO[1][tid]) + (sO[2][tid] + sO[3][tid]));
    if (O_SLEEP) __builtin_amdgcn_s_sleep(O_SLEEP);
    gu64* go = ws + WS_OG + (g * 8 + w) * 128 + lane;
    u64 ml = get(gm);
    u64 o0 = get(go), o1 = get(go + 64);
    __builtin_amdgcn_sched_barrier(0);
    f32x4 ya = {0.f, 0.f, 0.f, 0.f};
    MV<0, 2>::run(w2v, sH[8 * w + 4 * (c & 1) + r], mk4, ya);
    if (w == 3) {
        u32 spins = 0;
        if (!__all(okg(ml, thi, tlo))) {
            asm volatile("; retry ml" ::: "memory");
#pragma nounroll
            do {
                if (++spins > kSpinCap) { fail = true; break; }
                ml = get(gm);
            } while (!__all(okg(ml, thi, tlo)));
        }
        const float v = val(ml);
        const float M = row_max((c & 1) ? -INFINITY : v);
        const float e = __expf(dpp<0xA0>(v) - M);
        const float L = row_sum((c & 1) ? v * e : 0.f);
        if (lane < 16 && !(c & 1)) sS[c >> 1] = fail ? __builtin_nanf("") : e / L;
    }
    __builtin_amdgcn_sched_barrier(0);
    {
        u32 spins = 0;
        if (!__all(okg(o0, thi, tlo) & okg(o1, thi, tlo))) {
            asm volatile("; retry o" ::: "memory");
#pragma nounroll
            do {
                if (++spins > kSpinCap) { fail = true; break; }
                o0 = get(go); o1 = get(go + 64);
            } while (!__all(okg(o0, thi, tlo) & okg(o1, thi, tlo)));
        }
    }
    sOg[lane][w] = fail ? __builtin_nanf("") : val(o0); sOg[64 + lane][w] = val(o1);
    ST(6);
    bar();
    {
        const f32x2 ov = *(const f32x2*)&sOg[16 * w + 4 * (c & 3) + r][2 * (c >> 2)], sv = *(const f32x2*)&sS[2 * (c >> 2)];
        float a = ov[0] * sv[0] + ov[1] * sv[1];
        a += dpp<0x124>(a); a += dpp<0x128>(a);
        MV<0, 4>::run(wov, bfbits(a) * 0x10001u, mk4, ya);
    }
    if (lane < 32 && c < 10) *(f32x4*)&sP[w][8 * c + 4 * r] = ya;
    bar();
    ST(7);
    if (w < 5) {
        const int col = 16 * w + (lane >> 2), qd = lane & 3;
        float a = sP[2 * qd][col] + sP[2 * qd + 1][col];
        a += dpp<0xB1>(a); a += dpp<0x4E>(a);
        if (__builtin_amdgcn_readfirstlane(sOk) == 0u) {
            u32 spins = 0;
#pragma nounroll
            while (!okg(get(ws + WS_T + g * 16), thi, tlo) && ++spins < kSpinCap) { }
            asm volatile("" ::: "memory");
        }
        const bool bad = !(fabsf(a) < 4096.0f);
        const float ah = truncf(a * 16.0f);
        const long long fx = ((long long)(int)ah << 30) + (long long)(int)((a * 16.0f - ah) * 1073741824.0f);
        const u64 add = (1ull << 58) + (1ull << 46) + (bad ? (1ull << 52) : (u64)fx);
        if (qd == 0) {
            const u64 tot = __hip_atomic_fetch_add(ws + WS_Y + ocol, add, RLX_AGENT) + add;
            if ((tot >> 58) == 32u) {
                float y = (float)((long long)(tot & ((1ull << 52) - 1)) - (1ll << 51)) * 5.820766091346741e-11f;
                if ((tot >> 52) & 63u) y = __builtin_nanf("");
                out[ocol] = (unsigned short)bfbits(lo16(xo) + y + lo16(b2o));
                if (ocol == 0) __hip_atomic_store(ws, tg, RLX_AGENT);
            }
        }
    }
    ST(8);
}

extern "C" void kernel_launch(void* const* d_in, const int* in_sizes, int n_in, void* d_out, int out_size,
                              void* d_ws, size_t ws_size, hipStream_t stream) {
    typedef const unsigned short* P;
    fused_decode<<<256, 512, 0, stream>>>((P)d_in[0], (P)d_in[1], (P)d_in[2], (P)d_in[3], (P)d_in[4], (P)d_in[6],
                                          (P)d_in[7], (P)d_in[8], (P)d_in[9], (unsigned short*)d_out,
                                          (u64*)((char*)d_ws + WS_OFF(ws_size)));
}
```

```cpp
#include <hip/hip_runtime.h>
#include <hip/hip_bf16.h>

typedef unsigned u32;
typedef unsigned long long u64;
typedef u32 u32x2 __attribute__((ext_vector_type(2)));
typedef u32 u32x4 __attribute__((ext_vector_type(4)));
typedef float f32x2 __attribute__((ext_vector_type(2)));
typedef float f32x4 __attribute__((ext_vector_type(4)));
typedef __bf16 bf2 __attribute__((ext_vector_type(2)));
typedef __bf16 bf16x8 __attribute__((ext_vector_type(8)));
typedef short s16x4 __attribute__((ext_vector_type(4)));
typedef __attribute__((address_space(1))) u64 gu64;
typedef __amdgpu_buffer_rsrc_t rsrc_t;
typedef volatile __attribute__((address_space(3))) u32 lu32;
typedef volatile __attribute__((address_space(3))) u32x4 lu32x4;

#define RLX_AGENT __ATOMIC_RELAXED, __HIP_MEMORY_SCOPE_AGENT
#define AUX_KV 2
#define AUX_W 2
#define AUX_WO 2
#define GQ_SLEEP 10

constexpr int DM = 640, NH = 32, DH = 128, SK = 8192, WIN = 2048, FF = 1728, NQ = NH * DH;
constexpr u32 CODE_BYTES = 11008;
#define VSN 10
constexpr int VS = VSN;
constexpr u32 OOB = 0x80000000u;
constexpr u32 kSpinCap = 1u << 21;
constexpr int WS_QH = 512;
constexpr int WS_OG = WS_QH + 32 * 8 * 192;
constexpr int WS_ML = WS_OG + 32 * 8 * 128;
constexpr int WS_Y = WS_ML + 32 * 8 * 16;
constexpr int WS_T = WS_Y + 640;
constexpr int WS_END = WS_T + 32 * 16;

#define PACE 14
#define PT0 115
#define PDT 58
constexpr u32 T16 = PT0 + 16 * PDT / 4;
#define SLOT_T(n) ((n) < 16 ? (u32)(PT0 + (n) * PDT / 4) : T16 + ((u32)((n) - 16) * pdt2 >> 2))
#define SLOT(n) do { const u32 tt_ = SLOT_T(n); while ((u32)__builtin_amdgcn_s_memrealtime() - t0 < tt_) { } } while (0)
#define PACEW(n) do { if (((n) & 1) == 0) SLOT(n); if (PACE && (n) >= PACE) { asm volatile("s_waitcnt vmcnt(%0)" :: "i"(PACE - 1) : "memory"); } __builtin_amdgcn_sched_barrier(0); } while (0)
#define O_SLEEP 14
#define CODEPF 1
#define TEND 685
#define GQT 325
#define O_SK 2
#define ORGI 7

__device__ __forceinline__ rsrc_t mk(const void* p, u32 bytes) {
    return __builtin_amdgcn_make_buffer_rsrc((void*)p, 0, bytes, 0x00020000);
}
__device__ __forceinline__ float dot2(u32 a, u32 b, float c) {
    return __builtin_amdgcn_fdot2_f32_bf16(__builtin_bit_cast(bf2, a), __builtin_bit_cast(bf2, b), c, false);
}
__device__ __forceinline__ u32 bfbits(float f) { return (u32)__builtin_bit_cast(unsigned short, (__bf16)f); }
__device__ __forceinline__ float bfr(float f) { return __uint_as_float(bfbits(f) << 16); }
__device__ __forceinline__ float lo16(u32 v) { return __uint_as_float(v << 16); }
__device__ __forceinline__ float hi16(u32 v) { return __uint_as_float(v & 0xffff0000u); }
template <int C> __device__ __forceinline__ float dpp(float v) {
    return __uint_as_float(__builtin_amdgcn_update_dpp(0u, __float_as_uint(v), C, 0xf, 0xf, true));
}
__device__ __forceinline__ float row_sum(float v) {
    v += dpp<0xB1>(v); v += dpp<0x4E>(v); v += dpp<0x141>(v); v += dpp<0x140>(v); return v;
}
__device__ __forceinline__ float row_max(float v) {
    v = fmaxf(v, dpp<0xB1>(v)); v = fmaxf(v, dpp<0x4E>(v)); v = fmaxf(v, dpp<0x141>(v)); v = fmaxf(v, dpp<0x140>(v)); return v;
}
__device__ __forceinline__ float sw32(float a, float b) {
    auto r = __builtin_amdgcn_permlane32_swap(__float_as_uint(a), __float_as_uint(b), false, false);
    return __uint_as_float(r[0]) + __uint_as_float(r[1]);
}
__device__ __forceinline__ float sw16(float a, float b) {
    auto r = __builtin_amdgcn_permlane16_swap(__float_as_uint(a), __float_as_uint(b), false, false);
    return __uint_as_float(r[0]) + __uint_as_float(r[1]);
}
__device__ __forceinline__ float col_sum(float v) { v = sw32(v, v); return sw16(v, v); }
__device__ __forceinline__ float col_max(float v) {
    auto r = __builtin_amdgcn_permlane32_swap(__float_as_uint(v), __float_as_uint(v), false, false);
    v = fmaxf(__uint_as_float(r[0]), __uint_as_float(r[1]));
    auto s = __builtin_amdgcn_permlane16_swap(__float_as_uint(v), __float_as_uint(v), false, false);
    return fmaxf(__uint_as_float(s[0]), __uint_as_float(s[1]));
}
template <int C> __device__ __forceinline__ float rs(float a, float b, bool hi) {
    return (hi ? b : a) + dpp<C>(hi ? a : b);
}
template <int I> __device__ __forceinline__ u32 row_bcast(u32 v) {
    return __builtin_amdgcn_update_dpp(0u, v, 0x150 + I, 0xf, 0xf, true);
}
template <int I, int N> struct MV {
    static __device__ __forceinline__ void run(const u32x4 (&vv)[N], u32 pp, const u32x4& mk4, f32x4& acc) {
        const u32 p = row_bcast<I>(pp);
        const u32x4 a = {p & mk4[0], p & mk4[1], p & mk4[2], p & mk4[3]};
        acc = __builtin_amdgcn_mfma_f32_16x16x32_bf16(__builtin_bit_cast(bf16x8, a), __builtin_bit_cast(bf16x8, vv[I]), acc, 0, 0, 0);
        __builtin_amdgcn_sched_barrier(0);
        if constexpr (I + 1 < N) MV<I + 1, N>::run(vv, pp, mk4, acc);
    }
};
__device__ __forceinline__ void bar() {
    asm volatile("s_waitcnt lgkmcnt(0)" ::: "memory");
    __builtin_amdgcn_s_barrier();
    asm volatile("" ::: "memory");
    __builtin_amdgcn_sched_barrier(0);
}
__device__ __forceinline__ void put(gu64* p, u32 thi, u32 tlo, float v) {
    u32 b = ((__float_as_uint(v) + 0x20u) & ~0x3fu) | tlo;
    __hip_atomic_store(p, ((u64)thi << 32) | b, RLX_AGENT);
}
__device__ __forceinline__ u64 get(gu64* p) { return __hip_atomic_load(p, RLX_AGENT); }
__device__ __forceinline__ bool okg(u64 x, u32 thi, u32 tlo) { return ((((u32)(x >> 32)) ^ thi) | (((u32)x & 0x3fu) ^ tlo)) == 0u; }
__device__ __forceinline__ float val(u64 x) { return __uint_as_float((u32)x & ~0x3fu); }

#define EPOCH (*(const volatile __attribute__((address_space(4))) u64*)wsp)

__global__ void __launch_bounds__(512, 2)
fused_decode(const unsigned short* __restrict__ x, const unsigned short* __restrict__ kc,
             const unsigned short* __restrict__ vc, const unsigned short* __restrict__ wq,
             const unsigned short* __restrict__ wo, const unsigned short* __restrict__ w1,
             const unsigned short* __restrict__ b1, const unsigned short* __restrict__ w2,
             const unsigned short* __restrict__ b2, unsigned short* __restrict__ out, u64* wsp) {
    __shared__ float sA[4][192];
    __shared__ float sG[4][192];
    __shared__ float sO[4][128];
    __shared__ float sOg[128][8];
    __shared__ float sS[8];
    __shared__ float sP[8][88];
    __shared__ float sM[4], sL[4];
    __shared__ u32 sH[64];
    __shared__ u32x4 sArr, sRdy;
    __shared__ u32 sDt, sOk;
    u64 code0;
    asm volatile("s_getpc_b64 %0" : "=s"(code0));
    gu64* ws = (gu64*)wsp;
    const int tid = threadIdx.x, lane = tid & 63;
    const int w = __builtin_amdgcn_readfirstlane(tid >> 6);
    const int b = blockIdx.x, xcd = b & 7, rr = b >> 3, s4 = rr >> 3;
    const int g = xcd * 4 + s4, j = rr & 7;
    const int f = xcd < 5 ? (s4 < 3 ? 3 * xcd + s4 : -1) : 15 + 4 * (xcd - 5) + s4;
    const bool ffn = f >= 0;
    const int r = lane >> 4, c = lane & 15;
    const u32 t0 = (u32)__builtin_amdgcn_s_memrealtime();
    asm volatile("" :: "s"(x), "s"(kc), "s"(vc), "s"(wq), "s"(wo), "s"(w1), "s"(b1), "s"(w2), "s"(b2), "s"(out), "s"(wsp));
    const rsrc_t rx = mk(x, DM * 2);
    const rsrc_t rwo = mk(wo + (size_t)(g * DH + 16 * w) * DM + 80 * j, 15 * DM * 2 + 160);
    const rsrc_t rw2 = mk(w2 + (size_t)((ffn ? f : 0) * 64 + 8 * w) * DM + 80 * j, ffn ? 7 * DM * 2 + 160 : 0);
    const u32 wofs = c < 10 ? r * DM * 2 + c * 16 : OOB;
    u32x4 wov[4], w2v[2];
    const u32 hm = (c & 1) ? 0xffff0000u : 0x0000ffffu;
    const u32x4 mk4 = {(c >> 1) == 0 ? hm : 0u, (c >> 1) == 1 ? hm : 0u, (c >> 1) == 2 ? hm : 0u, (c >> 1) == 3 ? hm : 0u};
    const int ocol = 80 * j + 16 * w + (lane >> 2);
    u32 xo, b2o, thi, tlo;
    u64 tg;
    bool fail = false;
    u32 pdt2 = PDT;
    gu64* gm = ws + WS_ML + (g * 8 + (w == 3 ? c >> 1 : w)) * 16 + (c & 1);

    if (__builtin_expect(w < 4, 1)) {
        const int row0 = 80 * j + 20 * w;
        u32 xr[5]; u32x4 wqv[5]; u32x2 w1v[5];
        const rsrc_t rwq = mk(wq + (size_t)row0 * NQ + g * DH, 19 * NQ * 2 + 256);
        const rsrc_t rw1 = mk(w1 + (size_t)row0 * FF + (ffn ? f : 0) * 64, ffn ? 19 * FF * 2 + 128 : 0);
        const u32x4 xa = __builtin_amdgcn_raw_buffer_load_b128(rx, lane * 16, 0, 0);
        const u32x4 xb = __builtin_amdgcn_raw_buffer_load_b128(rx, 1024 + lane * 16, 0, 0);
#pragma unroll
        for (int i = 0; i < 5; ++i) xr[i] = __builtin_amdgcn_raw_buffer_load_b16(rx, (row0 + 4 * i + r) * 2, 0, 0);
        __builtin_amdgcn_sched_barrier(0);
#pragma unroll
        for (int i = 0; i < 5; ++i) wqv[i] = __builtin_amdgcn_raw_buffer_load_b128(rwq, (4 * i + r) * NQ * 2 + c * 16, 0, AUX_W);
        __builtin_amdgcn_sched_barrier(0);
#pragma unroll
        for (int i = 0; i < 5; ++i) w1v[i] = __builtin_amdgcn_raw_buffer_load_b64(rw1, (4 * i + r) * FF * 2 + c * 8, 0, AUX_W);
        __builtin_amdgcn_sched_barrier(0);
        tg = EPOCH + 1; tlo = (u32)tg & 0x3fu; thi = (u32)(tg >> 6);
        float xs[16];
#pragma unroll
        for (int i = 0; i < 4; ++i) { xs[2 * i] = lo16(xa[i]); xs[2 * i + 1] = hi16(xa[i]); xs[8 + 2 * i] = lo16(xb[i]); xs[9 + 2 * i] = hi16(xb[i]); }
        const float x0 = __uint_as_float(__builtin_amdgcn_readfirstlane(xa[0]) << 16);
        float s1 = 0.f, s2 = 0.f, t1 = 0.f, t2 = 0.f;
#pragma unroll
        for (int i = 0; i < 8; ++i) { const float d = xs[i] - x0, e = xs[8 + i] - x0; s1 += d; s2 += d * d; t1 += e; t2 += e * e; }
        s1 += lane < 16 ? t1 : 0.f; s2 += lane < 16 ? t2 : 0.f;
        const float m1 = col_sum(row_sum(s1)) * (1.0f / DM), m2 = col_sum(row_sum(s2)) * (1.0f / DM);
        const float mean = x0 + m1, var = bfr(fmaxf(m2 - m1 * m1, 0.f));
        const float mb = bfr(mean), sd = bfr(sqrtf(bfr(var + 1e-5f)));
        const float rsd = 1.0f / sd;
        u32x4 xm[5];
#pragma unroll
        for (int i = 0; i < 5; ++i) {
            const u32 xx = bfbits(bfr(lo16(xr[i]) - mb) * rsd) * 0x10001u;
            xm[i] = u32x4{xx & mk4[0], xx & mk4[1], xx & mk4[2], xx & mk4[3]};
        }
        __builtin_amdgcn_sched_barrier(0);
        f32x4 qa = {0.f, 0.f, 0.f, 0.f}, ha = {0.f, 0.f, 0.f, 0.f};
#pragma unroll
        for (int i = 0; i < 5; ++i)
            qa = __builtin_amdgcn_mfma_f32_16x16x32_bf16(__builtin_bit_cast(bf16x8, xm[i]), __builtin_bit_cast(bf16x8, wqv[i]), qa, 0, 0, 0);
#pragma unroll
        for (int i = 0; i < 5; ++i)
            ha = __builtin_amdgcn_mfma_f32_16x16x16bf16_1k(__builtin_bit_cast(s16x4, u32x2{xm[i][0], xm[i][1]}), __builtin_bit_cast(s16x4, w1v[i]), ha, 0, 0, 0);
        if (lane < 32) *(f32x4*)&sA[w][8 * c + 4 * r] = qa;
        if (lane < 16) *(f32x4*)&sA[w][128 + 4 * c] = ha;
        asm volatile("s_waitcnt lgkmcnt(0)" ::: "memory");
        ((lu32*)&sArr)[w] = (u32)tg;
        asm volatile("s_waitcnt lgkmcnt(0)" ::: "memory");
        const u32x4 arr = *(lu32x4*)&sArr;
        if (__builtin_amdgcn_readfirstlane((arr[0] ^ (u32)tg) | (arr[1] ^ (u32)tg) | (arr[2] ^ (u32)tg) | (arr[3] ^ (u32)tg)) == 0u) {
            asm volatile("" ::: "memory");
#pragma unroll
            for (int k = 0; k < 3; ++k) {
                const int e = 64 * k + lane;
                put(ws + WS_QH + (g * 8 + j) * 192 + e, thi, tlo, (sA[0][e] + sA[1][e]) + (sA[2][e] + sA[3][e]));
            }
        }
        const u32 b1v = __builtin_amdgcn_raw_buffer_load_b16(mk(b1 + (ffn ? f : 0) * 64, 128), lane * 2, 0, 0);
        xo = __builtin_amdgcn_raw_buffer_load_b16(rx, ocol * 2, 0, 0);
        b2o = __builtin_amdgcn_raw_buffer_load_b16(mk(b2, DM * 2), ocol * 2, 0, 0);
        __builtin_amdgcn_s_sleep(GQ_SLEEP);
        while ((u32)__builtin_amdgcn_s_memrealtime() - t0 < (u32)GQT) { }
        gu64* gq = ws + WS_QH + (g * 8 + 2 * w) * 192 + lane;
        u32 spins = 0;
        u64 q[6];
#pragma unroll
        for (int k = 0; k < 6; ++k) q[k] = get(gq + 64 * k);
        bool ok = true;
#pragma unroll
        for (int k = 0; k < 6; ++k) ok &= okg(q[k], thi, tlo);
        if (!__all(ok)) {
            asm volatile("; retry q" ::: "memory");
#pragma nounroll
            while (!__all(ok)) {
                ++spins;
                if (spins > kSpinCap) { q[0] = 0x7fc00000u; break; }
                ok = true;
#pragma unroll
                for (int k = 0; k < 6; ++k) q[k] = get(gq + 64 * k);
#pragma unroll
                for (int k = 0; k < 6; ++k) ok &= okg(q[k], thi, tlo);
            }
        }
#pragma unroll
        for (int k = 0; k < 3; ++k) sG[w][64 * k + lane] = val(q[k]) + val(q[k + 3]);
        asm volatile("s_waitcnt lgkmcnt(0)" ::: "memory");
        ((lu32*)&sRdy)[w] = (u32)tg;
        bar();
        pdt2 = __builtin_amdgcn_readfirstlane(sDt);
        const bool lateA = (u32)__builtin_amdgcn_s_memrealtime() - t0 >= SLOT_T(31);
        if (lateA) {
#pragma unroll
            for (int i = 0; i < 4; ++i) wov[i] = __builtin_amdgcn_raw_buffer_load_b128(rwo, wofs, i * 4 * DM * 2, AUX_WO);
            __builtin_amdgcn_sched_barrier(0);
#pragma unroll
            for (int i = 0; i < 2; ++i) w2v[i] = __builtin_amdgcn_raw_buffer_load_b128(rw2, wofs, i * 4 * DM * 2, AUX_WO);
            __builtin_amdgcn_sched_barrier(0);
        }
        bar();
        bar();
        if (w == 2) {
            const f32x4 m4 = *(const f32x4*)&sM[0], l4 = *(const f32x4*)&sL[0];
            const float m = fmaxf(fmaxf(m4[0], m4[1]), fmaxf(m4[2], m4[3]));
            if (lane < 2) put(ws + WS_ML + (g * 8 + j) * 16 + lane, thi, tlo, lane ? (l4[0] + l4[1]) + (l4[2] + l4[3]) : m);
        }
        if (w == 0) {
            const float a = (sG[0][128 + lane] + sG[1][128 + lane]) + (sG[2][128 + lane] + sG[3][128 + lane]);
            const float z = bfr(bfr(a) + lo16(b1v));
            const float u = 0.7978845608028654f * (z + 0.044715f * z * z * z);
            const float th = 1.0f - 2.0f / (__expf(2.0f * u) + 1.0f);
            sH[lane] = ffn ? bfbits(0.5f * z * (1.0f + th)) * 0x10001u : 0u;
        }
        if (!lateA) {
            SLOT(32);
#pragma unroll
            for (int i = 0; i < 4; ++i) wov[i] = __builtin_amdgcn_raw_buffer_load_b128(rwo, wofs, i * 4 * DM * 2, AUX_WO);
            __builtin_amdgcn_sched_barrier(0);
            SLOT(35);
#pragma unroll
            for (int i = 0; i < 2; ++i) w2v[i] = __builtin_amdgcn_raw_buffer_load_b128(rw2, wofs, i * 4 * DM * 2, AUX_WO);
            __builtin_amdgcn_sched_barrier(0);
        }
    } else {
        const size_t kvoff = ((size_t)g * SK + (SK - WIN) + 256 * j) * DH;
        const rsrc_t rk = mk(kc + kvoff, 65536), rv = mk(vc + kvoff, 65536);
        const int rot = (rr * 2 + xcd * 8 + (b & 1) + (w - 4) * 16) * 1024;
        u32x4 kv[16], vv[16];
        const u32x4 cpf = __builtin_amdgcn_raw_buffer_load_b128(mk((const void*)(code0 & ~(u64)255), CODEPF ? CODE_BYTES : 0),
                                                                lane * 16 + ((rr * 4 + w) & 15) * 1024, 0, 0);
        tg = EPOCH + 1; tlo = (u32)tg & 0x3fu; thi = (u32)(tg >> 6);
        if (b == 0 && w == 4) {
#pragma unroll
            for (int k = 0; k < 10; ++k) __hip_atomic_store(ws + WS_Y + 64 * k + lane, 0ull, RLX_AGENT);
            asm volatile("s_waitcnt vmcnt(0)" ::: "memory");
            if (lane < 32) __hip_atomic_store(ws + WS_T + lane * 16, ((u64)thi << 32) | (t0 << 6) | tlo, RLX_AGENT);
        }
        u64 org = 0;
#pragma unroll
        for (int i = 0; i < 16; ++i) {
            PACEW(i); kv[i] = __builtin_amdgcn_raw_buffer_load_b128(rk, lane * 16, (rot + i * 1024) & 0xffff, AUX_KV);
            if (i == ORGI) {
                org = get(ws + WS_T + g * 16);
                xo = __builtin_amdgcn_raw_buffer_load_b16(rx, ocol * 2, 0, 0);
                b2o = __builtin_amdgcn_raw_buffer_load_b16(mk(b2, DM * 2), ocol * 2, 0, 0);
            }
        }
        __builtin_amdgcn_sched_barrier(0);
        {
            const u32 ohi = __builtin_amdgcn_readfirstlane((u32)(org >> 32)), olo = __builtin_amdgcn_readfirstlane((u32)org);
            int d = (int)((t0 << 6) - (olo & ~0x3fu)) >> 6;
            const bool seen = ohi == thi && (olo & 0x3fu) == tlo;
            d = seen ? (d < 0 ? 0 : d > 150 ? 150 : d) : 0;
            pdt2 = (u32)((int)(TEND - T16) - d) / 5u;
            if (tid == 256) { sDt = pdt2; sOk = seen; }
        }
#pragma unroll
        for (int i = 0; i < VS; ++i) { PACEW(16 + i); vv[i] = __builtin_amdgcn_raw_buffer_load_b128(rv, lane * 16, (rot + i * 1024) & 0xffff, AUX_KV); }
        __builtin_amdgcn_sched_barrier(0);
        const u32x4 rdy = *(lu32x4*)&sRdy;
        const bool lateB = __builtin_amdgcn_readfirstlane((rdy[0] ^ (u32)tg) | (rdy[1] ^ (u32)tg) | (rdy[2] ^ (u32)tg) | (rdy[3] ^ (u32)tg)) != 0u;
        if (lateB) {
#pragma unroll
            for (int i = VS; i < 16; ++i) { PACEW(16 + i); vv[i] = __builtin_amdgcn_raw_buffer_load_b128(rv, lane * 16, (rot + i * 1024) & 0xffff, AUX_KV); }
            __builtin_amdgcn_sched_barrier(0);
        }
        asm volatile("" :: "v"(cpf));
        bar();
        u32 qb[4];
        {
            const int e = 8 * c + 2 * r;
            const f32x2 a = (*(const f32x2*)&sG[0][e] + *(const f32x2*)&sG[1][e]) + (*(const f32x2*)&sG[2][e] + *(const f32x2*)&sG[3][e]);
            const u32 pk = bfbits(a[0]) | (bfbits(a[1]) << 16);
            auto s = __builtin_amdgcn_permlane32_swap(pk, pk, false, false);
            auto s0 = __builtin_amdgcn_permlane16_swap(s[0], s[0], false, false);
            auto s1 = __builtin_amdgcn_permlane16_swap(s[1], s[1], false, false);
            qb[0] = s0[0]; qb[1] = s0[1]; qb[2] = s1[0]; qb[3] = s1[1];
        }
        float sc[16];
#pragma unroll
        for (int i = 0; i < 16; ++i) {
            float a = dot2(kv[i][0], qb[0], 0.f);
            a = dot2(kv[i][1], qb[1], a); a = dot2(kv[i][2], qb[2], a); sc[i] = dot2(kv[i][3], qb[3], a);
        }
        if (!lateB) {
#pragma unroll
            for (int i = VS; i < VS + 2; ++i) { PACEW(16 + i); vv[i] = __builtin_amdgcn_raw_buffer_load_b128(rv, lane * 16, (rot + i * 1024) & 0xffff, AUX_KV); }
        }
        __builtin_amdgcn_sched_barrier(0);
#pragma unroll
        for (int k = 0; k < 8; ++k) sc[k] = rs<0x140>(sc[k], sc[k + 8], c & 8);
#pragma unroll
        for (int k = 0; k < 4; ++k) sc[k] = rs<0x141>(sc[k], sc[k + 4], c & 4);
#pragma unroll
        for (int k = 0; k < 2; ++k) sc[k] = rs<0x4E>(sc[k], sc[k + 2], c & 2);
        const float s1 = bfr(bfr(rs<0xB1>(sc[0], sc[1], c & 1)) * 0.08838834764831845f);
        float mx = col_max(row_max(s1));
        if (lane == 0) sM[w - 4] = mx;
        if (!lateB) {
#pragma unroll
            for (int i = VS + 2; i < VS + 4; ++i) { PACEW(16 + i); vv[i] = __builtin_amdgcn_raw_buffer_load_b128(rv, lane * 16, (rot + i * 1024) & 0xffff, AUX_KV); }
        }
        __builtin_amdgcn_sched_barrier(0);
        bar();
        {
            const f32x4 m4 = *(const f32x4*)&sM[0];
            mx = fmaxf(fmaxf(m4[0], m4[1]), fmaxf(m4[2], m4[3]));
        }
        const float pf = __expf(s1 - mx);
        const u32 pp = bfbits(pf) * 0x10001u;
        const float ls = col_sum(row_sum(pf));
        if (lane == 0) sL[w - 4] = ls;
        bar();
        if (!lateB) {
#pragma unroll
            for (int i = VS + 4; i < 16; ++i) { PACEW(16 + i); vv[i] = __builtin_amdgcn_raw_buffer_load_b128(rv, lane * 16, (rot + i * 1024) & 0xffff, AUX_KV); }
        }
        __builtin_amdgcn_sched_barrier(0);
        {
            SLOT(33);
#pragma unroll
            for (int i = 0; i < 4; ++i) wov[i] = __builtin_amdgcn_raw_buffer_load_b128(rwo, wofs, i * 4 * DM * 2, AUX_WO);
            __builtin_amdgcn_sched_barrier(0);
            SLOT(36);
#pragma unroll
            for (int i = 0; i < 2; ++i) w2v[i] = __builtin_amdgcn_raw_buffer_load_b128(rw2, wofs, i * 4 * DM * 2, AUX_WO);
            __builtin_amdgcn_sched_barrier(0);
        }
        f32x4 acc = {0.f, 0.f, 0.f, 0.f};
        MV<0, 16>::run(vv, pp, mk4, acc);
        if (lane < 32) *(f32x4*)&sO[w - 4][8 * c + 4 * r] = acc;
    }
    bar();
    if (w < 2) put(ws + WS_OG + (g * 8 + j) * 128 + tid, thi, tlo, (sO[0][tid] + sO[1][tid]) + (sO[2][tid] + sO[3][tid]));
    if (tid == 320) { *(lu32x4*)&sArr = u32x4{~(u32)tg, ~(u32)tg, ~(u32)tg, ~(u32)tg}; *(lu32x4*)&sRdy = u32x4{~(u32)tg, ~(u32)tg, ~(u32)tg, ~(u32)tg}; }
    if (w < 2) __builtin_amdgcn_s_sleep(O_SLEEP - O_SK); else __builtin_amdgcn_s_sleep(O_SLEEP);
    gu64* go = ws + WS_OG + (g * 8 + w) * 128 + lane;
    u64 ml = get(gm);
    u64 o0 = get(go), o1 = get(go + 64);
    __builtin_amdgcn_sched_barrier(0);
    f32x4 ya = {0.f, 0.f, 0.f, 0.f};
    MV<0, 2>::run(w2v, sH[8 * w + 4 * (c & 1) + r], mk4, ya);
    if (w == 3) {
        u32 spins = 0;
        if (!__all(okg(ml, thi, tlo))) {
            asm volatile("; retry ml" ::: "memory");
#pragma nounroll
            do {
                if (++spins > kSpinCap) { fail = true; break; }
                ml = get(gm);
            } while (!__all(okg(ml, thi, tlo)));
        }
        const float v = val(ml);
        const float M = row_max((c & 1) ? -INFINITY : v);
        const float e = __expf(dpp<0xA0>(v) - M);
        const float L = row_sum((c & 1) ? v * e : 0.f);
        if (lane < 16 && !(c & 1)) sS[c >> 1] = fail ? __builtin_nanf("") : e / L;
    }
    __builtin_amdgcn_sched_barrier(0);
    {
        u32 spins = 0;
        if (!__all(okg(o0, thi, tlo) & okg(o1, thi, tlo))) {
            asm volatile("; retry o" ::: "memory");
#pragma nounroll
            do {
                if (++spins > kSpinCap) { fail = true; break; }
                o0 = get(go); o1 = get(go + 64);
            } while (!__all(okg(o0, thi, tlo) & okg(o1, thi, tlo)));
        }
    }
    sOg[lane][w] = fail ? __builtin_nanf("") : val(o0); sOg[64 + lane][w] = val(o1);
    bar();
    {
        const f32x2 ov = *(const f32x2*)&sOg[16 * w + 4 * (c & 3) + r][2 * (c >> 2)], sv = *(const f32x2*)&sS[2 * (c >> 2)];
        float a = ov[0] * sv[0] + ov[1] * sv[1];
        a += dpp<0x124>(a); a += dpp<0x128>(a);
        MV<0, 4>::run(wov, bfbits(a) * 0x10001u, mk4, ya);
    }
    if (lane < 32 && c < 10) *(f32x4*)&sP[w][8 * c + 4 * r] = ya;
    bar();
    if (w < 5) {
        const int col = 16 * w + (lane >> 2), qd = lane & 3;
        float a = sP[2 * qd][col] + sP[2 * qd + 1][col];
        a += dpp<0xB1>(a); a += dpp<0x4E>(a);
        if (__builtin_amdgcn_readfirstlane(sOk) == 0u) {
            u32 spins = 0;
#pragma nounroll
            while (!okg(get(ws + WS_T + g * 16), thi, tlo) && ++spins < kSpinCap) { }
            asm volatile("" ::: "memory");
        }
        const bool bad = !(fabsf(a) < 4096.0f);
        const float ah = truncf(a * 16.0f);
        const long long fx = ((long long)(int)ah << 30) + (long long)(int)((a * 16.0f - ah) * 1073741824.0f);
        const u64 add = (1ull << 58) + (1ull << 46) + (bad ? (1ull << 52) : (u64)fx);
        if (qd == 0) {
            const u64 tot = __hip_atomic_fetch_add(ws + WS_Y + ocol, add, RLX_AGENT) + add;
            if ((tot >> 58) == 32u) {
                float y = (float)((long long)(tot & ((1ull << 52) - 1)) - (1ll << 51)) * 5.820766091346741e-11f;
                if ((tot >> 52) & 63u) y = __builtin_nanf("");
                out[ocol] = (unsigned short)bfbits(lo16(xo) + y + lo16(b2o));
                if (ocol == 0) __hip_atomic_store(ws, tg, RLX_AGENT);
            }
        }
    }
}

extern "C" void kernel_launch(void* const* d_in, const int* in_sizes, int n_in, void* d_out, int out_size,
                              void* d_ws, size_t ws_size, hipStream_t stream) {
    typedef const unsigned short* P;
    fused_decode<<<256, 512, 0, stream>>>((P)d_in[0], (P)d_in[1], (P)d_in[2], (P)d_in[3], (P)d_in[4], (P)d_in[6],
                                          (P)d_in[7], (P)d_in[8], (P)d_in[9], (unsigned short*)d_out,
                                          (u64*)d_ws);
}
```
